```python
import torch

def _softmax(x, axis=-1):
    return torch.softmax(x, dim=axis)
def _gelu(x):
    return torch.nn.functional.gelu(x, approximate="tanh")
def _silu(x):
    return torch.nn.functional.silu(x)
def _sigmoid(x):
    return torch.sigmoid(x)
def _layernorm(x, axis=-1, eps=1e-5):
    m = x.mean(dim=axis, keepdim=True)
    v = x.var(dim=axis, keepdim=True, unbiased=False)
    return (x - m) / torch.sqrt(v + eps)
def _scatter_add(tgt, idx, src, axis=0):
    return tgt.clone().index_add_(axis, idx, src)
def _segment_sum(src, seg, n):
    out = torch.zeros((n,) + src.shape[1:], dtype=src.dtype)
    return out.index_add_(0, seg, src)
def _pad(x, pw):
    flat = tuple(v for pair in reversed(pw) for v in pair)
    return torch.nn.functional.pad(x, flat)

kernel_name = 'standalone__paged_block_table_kv_radixattention__sampling'

def setup_inputs():

    _w = (lambda a: (lambda t: t.to(torch.bfloat16) if t.is_floating_point() else t)(torch.as_tensor(a)))
    B, H, Hkv, Dh = 8, 32, 32, 192
    Lk = 4096
    bs = 16
    nb = max(2, Lk // bs)
    P = max(1, nb // 2)
    n_phys = P + B * (nb - P)
    shared = torch.arange(P, dtype=torch.int64)
    priv = P + torch.arange(B * (nb - P), dtype=torch.int64).reshape(B, nb - P)
    bt = torch.cat([torch.tile(shared[None, :], (B, 1)), priv], dim=1)
    ctx = nb * bs - (torch.arange(1, B + 1, dtype=torch.int64) * 2654435761) % max(1, (nb * bs - P * bs) // 2)
    return {
        "q": _w((lambda *s: torch.randn(*s, dtype=torch.bfloat16) * 0.8)(B, H, Dh)),
        "k_cache": _w((lambda *s: torch.randn(*s, dtype=torch.bfloat16) * 0.8)(n_phys, bs, Hkv, Dh)),
        "v_cache": _w((lambda *s: torch.randn(*s, dtype=torch.bfloat16) * 0.05)(n_phys, bs, Hkv, Dh)),
        "block_table": _w(bt.to(torch.int64)),
        "ctx_lens": _w(torch.as_tensor(ctx).to(torch.int64)),
    }

def reference(q, k_cache, v_cache, block_table, ctx_lens):
    B, H, Dh = q.shape
    n_phys, bs, Hkv, _ = k_cache.shape
    nb = block_table.shape[1]
    Lk = nb * bs
    rep = H // Hkv
    k = k_cache[block_table].reshape(B, Lk, Hkv, Dh)
    v = v_cache[block_table].reshape(B, Lk, Hkv, Dh)
    k = (lambda x, n, axis: torch.repeat_interleave(x, n, dim=axis))(k, rep, axis=2)
    v = (lambda x, n, axis: torch.repeat_interleave(x, n, dim=axis))(v, rep, axis=2)
    scores = torch.einsum("bhd,bkhd->bhk", q, k) * (Dh ** -0.5)
    pos = torch.arange(Lk)
    valid = pos[None, :] < ctx_lens[:, None]
    scores = torch.where(valid[:, None, :], scores, -1e9)
    p = _softmax(scores, axis=-1)
    return torch.einsum("bhk,bkhd->bhd", p, v)
```

```cpp
#include <hip/hip_runtime.h>
#include <hip/hip_bf16.h>
#include <cstdio>
#include <cstdint>

#define KW 8
#define KNS 3
#define KNT 1

constexpr int B = 8, H = 32, D = 192, NB = 256, BS = 16, LK = NB * BS;
constexpr int ROWB = D * 2;
constexpr int TOKB = H * ROWB;
constexpr int PAGEB = BS * TOKB;
constexpr int NSPLIT = 8;
constexpr int W = KW, NS = KNS, NTHR = W * 64;
constexpr int TILEB = BS * ROWB;
constexpr int RINGB = NS * TILEB;
constexpr int LDS_LIST = W * RINGB;
constexpr int LDS_CNT = LDS_LIST + 256 * 8;
constexpr int LDS_BYTES = LDS_CNT + 32 * 4;
static_assert(LDS_BYTES <= 160 * 1024, "LDS");
static_assert(RINGB >= 8 * D * 4 + 64, "merge area");
constexpr float SCALE_LOG2E = 0.07216878364870322f * 1.4426950408889634f;

#define LAS __attribute__((address_space(3)))
typedef __attribute__((ext_vector_type(8))) __bf16 bf16x8;
typedef __attribute__((ext_vector_type(4))) __bf16 bf16x4;
typedef __attribute__((ext_vector_type(4))) short s16x4;
typedef __attribute__((ext_vector_type(4))) float f32x4;
typedef __attribute__((ext_vector_type(2))) float f32x2;
typedef __attribute__((ext_vector_type(4))) unsigned u32x4;

__device__ __forceinline__ float xrow16_max(float x) {
  auto s = __builtin_amdgcn_permlane16_swap(__float_as_uint(x), __float_as_uint(x), false, false);
  x = fmaxf(__uint_as_float(s[0]), __uint_as_float(s[1]));
  auto t = __builtin_amdgcn_permlane32_swap(__float_as_uint(x), __float_as_uint(x), false, false);
  return fmaxf(__uint_as_float(t[0]), __uint_as_float(t[1]));
}
__device__ __forceinline__ float xrow16_sum(float x) {
  auto s = __builtin_amdgcn_permlane16_swap(__float_as_uint(x), __float_as_uint(x), false, false);
  x = __uint_as_float(s[0]) + __uint_as_float(s[1]);
  auto t = __builtin_amdgcn_permlane32_swap(__float_as_uint(x), __float_as_uint(x), false, false);
  return __uint_as_float(t[0]) + __uint_as_float(t[1]);
}

#define NTS " nt"
__device__ __forceinline__ void dma_tile(__amdgpu_buffer_rsrc_t srd, unsigned voff, unsigned soff, unsigned m0v) {
  const unsigned soff1 = soff + 8u * TOKB;
  asm volatile(
      "s_mov_b32 m0, %0\n\ts_nop 0\n\t"
      "buffer_load_dwordx4 %1, %2, %3 offen" NTS " lds\n\t"
      "s_add_u32 m0, m0, 1024\n\ts_nop 0\n\t"
      "buffer_load_dwordx4 %1, %2, %4 offen" NTS " lds\n\t"
      "s_add_u32 m0, m0, 896\n\ts_nop 0\n\t"
      "buffer_load_dwordx4 %1, %2, %3 offen offset:128" NTS " lds\n\t"
      "s_add_u32 m0, m0, 1024\n\ts_nop 0\n\t"
      "buffer_load_dwordx4 %1, %2, %4 offen offset:128" NTS " lds\n\t"
      "s_add_u32 m0, m0, 896\n\ts_nop 0\n\t"
      "buffer_load_dwordx4 %1, %2, %3 offen offset:256" NTS " lds\n\t"
      "s_add_u32 m0, m0, 1024\n\ts_nop 0\n\t"
      "buffer_load_dwordx4 %1, %2, %4 offen offset:256" NTS " lds"
      :: "s"(m0v), "v"(voff), "s"(srd), "s"(soff), "s"(soff1) : "memory", "scc");
}
template <int N> __device__ __forceinline__ void wait_vm() { asm volatile("s_waitcnt vmcnt(%0)" :: "i"(N) : "memory"); }
__device__ __forceinline__ void wait_tiles(int rem) {
  if (rem >= NS - 1) wait_vm<6 * (NS - 1)>();
  else if (NS > 5 && rem == 4) wait_vm<24>();
  else if (NS > 4 && rem == 3) wait_vm<18>();
  else if (NS > 3 && rem == 2) wait_vm<12>();
  else if (NS > 2 && rem == 1) wait_vm<6>();
  else wait_vm<0>();
}

__global__ void __launch_bounds__(NTHR, NTHR / 256) attn_main(const __hip_bfloat16* __restrict__ q, const __hip_bfloat16* __restrict__ kc,
                                                   const __hip_bfloat16* __restrict__ vc, const int* __restrict__ bt,
                                                   const int* __restrict__ ctxl, float* __restrict__ part, float* __restrict__ pml, int nphys) {
  __shared__ __attribute__((aligned(16))) char lds[LDS_BYTES];
  const int tid = threadIdx.x, lane = tid & 63, wave = __builtin_amdgcn_readfirstlane(tid >> 6);
  const int s = blockIdx.x & (NSPLIT - 1), h = blockIdx.x / NSPLIT;
  const int g = lane >> 4, c16 = lane & 15;

  int pb[B];
  int ce[B];
  if (tid < NB) {
#pragma unroll
    for (int b = 0; b < B; ++b) pb[b] = bt[b * NB + tid];
  } else {
#pragma unroll
    for (int b = 0; b < B; ++b) pb[b] = 0;
  }
  const int myctx_raw = (c16 < B) ? ctxl[c16] : 1;
  u32x4 qf[6];
#pragma unroll
  for (int i = 0; i < 6; ++i) {
    qf[i] = u32x4{0u, 0u, 0u, 0u};
    if (c16 < B) qf[i] = *reinterpret_cast<const u32x4*>(q + ((size_t)c16 * H + h) * D + 32 * i + 8 * g);
  }
#pragma unroll
  for (int b = 0; b < B; ++b) { const int c = ctxl[b]; ce[b] = (c <= 0 || c > LK) ? LK : c; }
  if (myctx_raw <= 0) {
#pragma unroll
    for (int i = 0; i < 6; ++i) qf[i] = u32x4{0u, 0u, 0u, 0u};
  }
  const int myctx = (myctx_raw <= 0 || myctx_raw > LK) ? LK : myctx_raw;

  unsigned long long bal[B];
  unsigned msk[B];
  int* cnt = reinterpret_cast<int*>(lds + LDS_CNT);
  {
    unsigned live = 0;
#pragma unroll
    for (int b = 0; b < B; ++b) { if (pb[b] < 0) pb[b] += nphys; live |= (tid < NB && tid * BS < ce[b]) ? (1u << b) : 0u; }
#pragma unroll
    for (int b = 0; b < B; ++b) {
      unsigned m = 0;
#pragma unroll
      for (int b2 = 0; b2 < B; ++b2) m |= (pb[b2] == pb[b]) ? (1u << b2) : 0u;
      m &= live;
      const bool leader = ((live >> b) & 1u) && (m & ((1u << b) - 1u)) == 0u;
      msk[b] = leader ? m : 0u;
      bal[b] = __ballot(leader);
      if (lane == 0 && wave < 4) cnt[b * 4 + wave] = __popcll(bal[b]);
    }
  }
  __syncthreads();
  int nloc;
  {
    int run = 0, base[B];
#pragma unroll
    for (int b = 0; b < B; ++b) {
#pragma unroll
      for (int w = 0; w < 4; ++w) { if (w == wave) base[b] = run; run += cnt[b * 4 + w]; }
    }
    const int lo = (run * s) / NSPLIT, hi = (run * (s + 1)) / NSPLIT;
    nloc = hi - lo;
    if (wave < 4) {
      const unsigned long long lt = (1ull << lane) - 1ull;
#pragma unroll
      for (int b = 0; b < B; ++b) {
        const int pos = base[b] + __popcll(bal[b] & lt) - lo;
        if (msk[b] != 0u && pos >= 0 && pos < nloc)
          *reinterpret_cast<int2*>(lds + LDS_LIST + pos * 8) = make_int2(pb[b], tid | (int)(msk[b] << 16));
      }
    }
  }
  __syncthreads();
  nloc = __builtin_amdgcn_readfirstlane(nloc);
  const int nw = (nloc > wave) ? (nloc - wave + W - 1) / W : 0;
  int ephys = 0, emeta = 0;
  if (lane < nw) { const int2 e = *reinterpret_cast<const int2*>(lds + LDS_LIST + (wave + W * lane) * 8); ephys = e.x; emeta = e.y; }
#pragma unroll
  for (int i = 0; i < 6; ++i) asm volatile("" : "+v"(qf[i]));
  asm volatile("" : "+v"(ephys), "+v"(emeta));

  const unsigned kvbytes = (unsigned)nphys * (unsigned)PAGEB;
  const __amdgpu_buffer_rsrc_t srdK = __builtin_amdgcn_make_buffer_rsrc((void*)kc, (short)0, (int)kvbytes, 0x00020000);
  const __amdgpu_buffer_rsrc_t srdV = __builtin_amdgcn_make_buffer_rsrc((void*)vc, (short)0, (int)kvbytes, 0x00020000);
  const unsigned ldsbase = (unsigned)(size_t)(LAS char*)lds;
  const unsigned ring = ldsbase + (unsigned)wave * RINGB;
  const unsigned voff = (unsigned)(lane >> 3) * TOKB + (unsigned)(((lane & 7) ^ ((lane >> 3) & 6)) * 16);
  const unsigned hoff = (unsigned)h * ROWB;
  unsigned kaddr[2], vaddr[4];
#pragma unroll
  for (int bb = 0; bb < 2; ++bb) kaddr[bb] = ring + c16 * 128 + (((4 * bb + g) ^ (c16 & 6)) * 16);
  {
    const int t = 4 * g + (c16 >> 2), p = lane & 3;
#pragma unroll
    for (int k = 0; k < 4; ++k) vaddr[k] = ring + t * 128 + ((((2 * k) ^ (t & 6)) + (p >> 1)) * 16) + (p & 1) * 8;
  }
  float m = -INFINITY, l = 0.f;
  f32x4 acc[12];
#pragma unroll
  for (int i = 0; i < 12; ++i) acc[i] = f32x4{0.f, 0.f, 0.f, 0.f};

  const int ntiles = 2 * nw;
  auto issue = [&](int T, unsigned slot) {
    const int phys = __builtin_amdgcn_readlane(ephys, T >> 1);
    const unsigned soff = (unsigned)phys * (unsigned)PAGEB + hoff;
    if (T & 1) dma_tile(srdV, voff, soff, ring + slot); else dma_tile(srdK, voff, soff, ring + slot);
  };
#pragma unroll
  for (int T = 0; T < NS; ++T) if (T < ntiles) issue(T, T * TILEB);
  unsigned slot = 0;
  for (int i = 0; i < nw; ++i) {
    const int meta = __builtin_amdgcn_readlane(emeta, i);
    const int col = meta & 0xffff;
    wait_tiles(ntiles - 1 - 2 * i);
    bf16x8 kf[6];
#pragma unroll
    for (int u = 0; u < 6; ++u) kf[u] = *(const LAS bf16x8*)(size_t)(kaddr[u & 1] + slot + (u >> 1) * 2048);
    asm volatile("s_waitcnt lgkmcnt(0)" : "+v"(kf[0]), "+v"(kf[1]), "+v"(kf[2]), "+v"(kf[3]), "+v"(kf[4]), "+v"(kf[5]) :: "memory");
    if (2 * i + NS < ntiles) issue(2 * i + NS, slot);
    slot = (slot + TILEB == RINGB) ? 0u : slot + TILEB;
    f32x4 s0 = {0.f, 0.f, 0.f, 0.f}, s1 = {0.f, 0.f, 0.f, 0.f};
#pragma unroll
    for (int u = 0; u < 6; u += 2) {
      s0 = __builtin_amdgcn_mfma_f32_16x16x32_bf16(kf[u], __builtin_bit_cast(bf16x8, qf[u]), s0, 0, 0, 0);
      s1 = __builtin_amdgcn_mfma_f32_16x16x32_bf16(kf[u + 1], __builtin_bit_cast(bf16x8, qf[u + 1]), s1, 0, 0, 0);
    }
    const int nval = ((meta >> (16 + c16)) & 1) ? (myctx - col * BS - 4 * g) : 0;
    float sc[4];
#pragma unroll
    for (int r = 0; r < 4; ++r) sc[r] = (r < nval) ? (s0[r] + s1[r]) * SCALE_LOG2E : -INFINITY;
    float tm = fmaxf(fmaxf(sc[0], sc[1]), fmaxf(sc[2], sc[3]));
    tm = xrow16_max(tm);
    const float mn = fmaxf(m, tm);
    const float msub = (mn == -INFINITY) ? 0.f : mn;
    const float alpha = __builtin_amdgcn_exp2f(m - msub);
    float p[4];
#pragma unroll
    for (int r = 0; r < 4; ++r) p[r] = __builtin_amdgcn_exp2f(sc[r] - msub);
    l = l * alpha + ((p[0] + p[1]) + (p[2] + p[3]));
    if (__any(mn > m)) {
#pragma unroll
      for (int dt = 0; dt < 12; ++dt) acc[dt] *= alpha;
    }
    m = mn;
    const bf16x4 pb4 = {(__bf16)p[0], (__bf16)p[1], (__bf16)p[2], (__bf16)p[3]};
    const s16x4 pf = __builtin_bit_cast(s16x4, pb4);
    wait_tiles(ntiles - 2 - 2 * i);
    s16x4 vf[12];
#pragma unroll
    for (int dt = 0; dt < 12; ++dt)
      vf[dt] = __builtin_amdgcn_ds_read_tr16_b64_v4i16((LAS s16x4*)(size_t)(vaddr[dt & 3] + slot + (dt >> 2) * 2048));
    asm volatile("s_waitcnt lgkmcnt(0)"
                 : "+v"(vf[0]), "+v"(vf[1]), "+v"(vf[2]), "+v"(vf[3]), "+v"(vf[4]), "+v"(vf[5]), "+v"(vf[6]), "+v"(vf[7]), "+v"(vf[8]), "+v"(vf[9]), "+v"(vf[10]), "+v"(vf[11])
                 :: "memory");
    if (2 * i + 1 + NS < ntiles) issue(2 * i + 1 + NS, slot);
    slot = (slot + TILEB == RINGB) ? 0u : slot + TILEB;
#pragma unroll
    for (int dt = 0; dt < 12; ++dt) acc[dt] = __builtin_amdgcn_mfma_f32_16x16x16bf16_1k(vf[dt], pf, acc[dt], 0, 0, 0);
  }
  l = xrow16_sum(l);
  if (c16 < B) {
    float* dst = reinterpret_cast<float*>(lds + wave * RINGB) + c16 * D + 4 * g;
#pragma unroll
    for (int dt = 0; dt < 12; ++dt) *reinterpret_cast<f32x4*>(dst + 16 * dt) = acc[dt];
    if (g == 0) *reinterpret_cast<f32x2*>(lds + wave * RINGB + B * D * 4 + c16 * 8) = f32x2{m, l};
  }
  __syncthreads();
  for (int it = tid; it < B * (D / 4); it += NTHR) {
    const int qq = it / (D / 4), d4 = it % (D / 4);
    float mw[W], lw[W], M = -INFINITY;
#pragma unroll
    for (int w = 0; w < W; ++w) { const f32x2 t = *reinterpret_cast<const f32x2*>(lds + w * RINGB + B * D * 4 + qq * 8); mw[w] = t[0]; lw[w] = t[1]; M = fmaxf(M, mw[w]); }
    float L = 0.f; f32x4 o = {0.f, 0.f, 0.f, 0.f};
#pragma unroll
    for (int w = 0; w < W; ++w) {
      const float wt = (lw[w] > 0.f) ? __builtin_amdgcn_exp2f(mw[w] - M) : 0.f;
      const f32x4 a = *reinterpret_cast<const f32x4*>(lds + w * RINGB + (qq * D + 4 * d4) * 4);
      L = fmaf(wt, lw[w], L);
      o += wt * a;
    }
    const size_t slotp = ((size_t)(h * B + qq) * NSPLIT + s);
    if (L > 0.f) *reinterpret_cast<f32x4*>(part + slotp * D + 4 * d4) = o;
    if (d4 == 0) *reinterpret_cast<f32x2*>(pml + slotp * 2) = f32x2{M, L};
  }
}

__global__ void __launch_bounds__(64) attn_combine(const float* __restrict__ part, const float* __restrict__ pml, __hip_bfloat16* __restrict__ out) {
  const int h = blockIdx.x / B, b = blockIdx.x % B, t = threadIdx.x;
  if (t >= D / 4) return;
  const size_t base = (size_t)(h * B + b) * NSPLIT;
  f32x2 ml[NSPLIT]; f32x4 a[NSPLIT];
#pragma unroll
  for (int s = 0; s < NSPLIT; ++s) { ml[s] = *reinterpret_cast<const f32x2*>(pml + (base + s) * 2); a[s] = *reinterpret_cast<const f32x4*>(part + (base + s) * D + 4 * t); }
  float M = -INFINITY;
#pragma unroll
  for (int s = 0; s < NSPLIT; ++s) M = fmaxf(M, ml[s][0]);
  float L = 0.f; f32x4 o = {0.f, 0.f, 0.f, 0.f};
#pragma unroll
  for (int s = 0; s < NSPLIT; ++s) {
    const bool on = ml[s][1] > 0.f;
    const float wt = on ? __builtin_amdgcn_exp2f(ml[s][0] - M) : 0.f;
    L = fmaf(wt, ml[s][1], L);
#pragma unroll
    for (int k = 0; k < 4; ++k) o[k] += on ? wt * a[s][k] : 0.f;
  }
  const float inv = 1.f / L;
  const bf16x4 r = {(__bf16)(o[0] * inv), (__bf16)(o[1] * inv), (__bf16)(o[2] * inv), (__bf16)(o[3] * inv)};
  *reinterpret_cast<bf16x4*>(out + ((size_t)b * H + h) * D + 4 * t) = r;
}

extern "C" void kernel_launch(void* const* d_in, const int* in_sizes, int n_in, void* d_out, int out_size, void* d_ws, size_t ws_size,
                              hipStream_t stream) {
  const __hip_bfloat16* q = (const __hip_bfloat16*)d_in[0];
  const __hip_bfloat16* k = (const __hip_bfloat16*)d_in[1];
  const __hip_bfloat16* v = (const __hip_bfloat16*)d_in[2];
  const int* bt = (const int*)d_in[3];
  const int* ctx = (const int*)d_in[4];
  const int nphys = in_sizes[1] / (BS * H * D);
  float* part = (float*)d_ws;
  float* pml = part + (size_t)H * B * NSPLIT * D;
  attn_main<<<H * NSPLIT, NTHR, 0, stream>>>(q, k, v, bt, ctx, part, pml, nphys);
  attn_combine<<<H * B, 64, 0, stream>>>(part, pml, (__hip_bfloat16*)d_out);
}
```

```cpp
#include <hip/hip_runtime.h>
#include <hip/hip_bf16.h>
#include <cstdio>
#include <cstdint>

#define KW 8
#define KNS 3
#define KNT 1

constexpr int B = 8, H = 32, D = 192, NB = 256, BS = 16, LK = NB * BS;
constexpr int ROWB = D * 2;
constexpr int TOKB = H * ROWB;
constexpr int PAGEB = BS * TOKB;
constexpr int NSPLIT = 8;
constexpr int W = KW, NS = KNS, NTHR = W * 64;
constexpr int TILEB = BS * ROWB;
constexpr int RINGB = NS * TILEB;
constexpr int LDS_LIST = W * RINGB;
constexpr int LDS_CNT = LDS_LIST + 256 * 8;
constexpr int LDS_BYTES = LDS_CNT + 32 * 4;
static_assert(LDS_BYTES <= 160 * 1024, "LDS");
static_assert(RINGB >= 8 * D * 4 + 64, "merge area");
constexpr float SCALE_LOG2E = 0.07216878364870322f * 1.4426950408889634f;

#define LAS __attribute__((address_space(3)))
typedef __attribute__((ext_vector_type(8))) __bf16 bf16x8;
typedef __attribute__((ext_vector_type(4))) __bf16 bf16x4;
typedef __attribute__((ext_vector_type(4))) short s16x4;
typedef __attribute__((ext_vector_type(4))) float f32x4;
typedef __attribute__((ext_vector_type(2))) float f32x2;
typedef __attribute__((ext_vector_type(4))) unsigned u32x4;

__device__ __forceinline__ float xrow16_max(float x) {
  auto s = __builtin_amdgcn_permlane16_swap(__float_as_uint(x), __float_as_uint(x), false, false);
  x = fmaxf(__uint_as_float(s[0]), __uint_as_float(s[1]));
  auto t = __builtin_amdgcn_permlane32_swap(__float_as_uint(x), __float_as_uint(x), false, false);
  return fmaxf(__uint_as_float(t[0]), __uint_as_float(t[1]));
}
__device__ __forceinline__ float xrow16_sum(float x) {
  auto s = __builtin_amdgcn_permlane16_swap(__float_as_uint(x), __float_as_uint(x), false, false);
  x = __uint_as_float(s[0]) + __uint_as_float(s[1]);
  auto t = __builtin_amdgcn_permlane32_swap(__float_as_uint(x), __float_as_uint(x), false, false);
  return __uint_as_float(t[0]) + __uint_as_float(t[1]);
}

#define KPOL " nt"
#define VPOL " nt"
#define DMA_TILE_ASM(POL) \
  asm volatile( \
      "s_mov_b32 m0, %0\n\ts_nop 0\n\t" \
      "buffer_load_dwordx4 %1, %2, %3 offen" POL " lds\n\t" \
      "s_add_u32 m0, m0, 1024\n\ts_nop 0\n\t" \
      "buffer_load_dwordx4 %1, %2, %4 offen" POL " lds\n\t" \
      "s_add_u32 m0, m0, 896\n\ts_nop 0\n\t" \
      "buffer_load_dwordx4 %1, %2, %3 offen offset:128" POL " lds\n\t" \
      "s_add_u32 m0, m0, 1024\n\ts_nop 0\n\t" \
      "buffer_load_dwordx4 %1, %2, %4 offen offset:128" POL " lds\n\t" \
      "s_add_u32 m0, m0, 896\n\ts_nop 0\n\t" \
      "buffer_load_dwordx4 %1, %2, %3 offen offset:256" POL " lds\n\t" \
      "s_add_u32 m0, m0, 1024\n\ts_nop 0\n\t" \
      "buffer_load_dwordx4 %1, %2, %4 offen offset:256" POL " lds" \
      :: "s"(m0v), "v"(voff), "s"(srd), "s"(soff), "s"(soff1) : "memory", "scc")
template <int ISV>
__device__ __forceinline__ void dma_tile(__amdgpu_buffer_rsrc_t srd, unsigned voff, unsigned soff, unsigned m0v) {
  const unsigned soff1 = soff + 8u * TOKB;
  if constexpr (ISV) DMA_TILE_ASM(VPOL); else DMA_TILE_ASM(KPOL);
}
template <int N> __device__ __forceinline__ void wait_vm() { asm volatile("s_waitcnt vmcnt(%0)" :: "i"(N) : "memory"); }
__device__ __forceinline__ void wait_tiles(int rem) {
  if (rem >= NS - 1) wait_vm<6 * (NS - 1)>();
  else if (NS > 5 && rem == 4) wait_vm<24>();
  else if (NS > 4 && rem == 3) wait_vm<18>();
  else if (NS > 3 && rem == 2) wait_vm<12>();
  else if (NS > 2 && rem == 1) wait_vm<6>();
  else wait_vm<0>();
}

__global__ void __launch_bounds__(NTHR, NTHR / 256) attn_main(const __hip_bfloat16* __restrict__ q, const __hip_bfloat16* __restrict__ kc,
                                                   const __hip_bfloat16* __restrict__ vc, const int* __restrict__ bt,
                                                   const int* __restrict__ ctxl, float* __restrict__ part, float* __restrict__ pml, int nphys) {
  __shared__ __attribute__((aligned(16))) char lds[LDS_BYTES];
  const int tid = threadIdx.x, lane = tid & 63, wave = __builtin_amdgcn_readfirstlane(tid >> 6);
  const int s = blockIdx.x & (NSPLIT - 1), h = blockIdx.x / NSPLIT;
  const int g = lane >> 4, c16 = lane & 15;

  int pb[B];
  int ce[B];
  if (tid < NB) {
#pragma unroll
    for (int b = 0; b < B; ++b) pb[b] = bt[b * NB + tid];
  } else {
#pragma unroll
    for (int b = 0; b < B; ++b) pb[b] = 0;
  }
  const int myctx_raw = (c16 < B) ? ctxl[c16] : 1;
  u32x4 qf[6];
#pragma unroll
  for (int i = 0; i < 6; ++i) {
    qf[i] = u32x4{0u, 0u, 0u, 0u};
    if (c16 < B) qf[i] = *reinterpret_cast<const u32x4*>(q + ((size_t)c16 * H + h) * D + 32 * i + 8 * g);
  }
#pragma unroll
  for (int b = 0; b < B; ++b) { const int c = ctxl[b]; ce[b] = (c <= 0 || c > LK) ? LK : c; }
  if (myctx_raw <= 0) {
#pragma unroll
    for (int i = 0; i < 6; ++i) qf[i] = u32x4{0u, 0u, 0u, 0u};
  }
  const int myctx = (myctx_raw <= 0 || myctx_raw > LK) ? LK : myctx_raw;

  unsigned long long bal[B];
  unsigned msk[B];
  int* cnt = reinterpret_cast<int*>(lds + LDS_CNT);
  {
    unsigned live = 0;
#pragma unroll
    for (int b = 0; b < B; ++b) { if (pb[b] < 0) pb[b] += nphys; live |= (tid < NB && tid * BS < ce[b]) ? (1u << b) : 0u; }
#pragma unroll
    for (int b = 0; b < B; ++b) {
      unsigned m = 0;
#pragma unroll
      for (int b2 = 0; b2 < B; ++b2) m |= (pb[b2] == pb[b]) ? (1u << b2) : 0u;
      m &= live;
      const bool leader = ((live >> b) & 1u) && (m & ((1u << b) - 1u)) == 0u;
      msk[b] = leader ? m : 0u;
      bal[b] = __ballot(leader);
      if (lane == 0 && wave < 4) cnt[b * 4 + wave] = __popcll(bal[b]);
    }
  }
  __syncthreads();
  int nloc;
  {
    int run = 0, base[B];
#pragma unroll
    for (int b = 0; b < B; ++b) {
#pragma unroll
      for (int w = 0; w < 4; ++w) { if (w == wave) base[b] = run; run += cnt[b * 4 + w]; }
    }
    const int lo = (run * s) / NSPLIT, hi = (run * (s + 1)) / NSPLIT;
    nloc = hi - lo;
    if (wave < 4) {
      const unsigned long long lt = (1ull << lane) - 1ull;
#pragma unroll
      for (int b = 0; b < B; ++b) {
        const int pos = base[b] + __popcll(bal[b] & lt) - lo;
        if (msk[b] != 0u && pos >= 0 && pos < nloc)
          *reinterpret_cast<int2*>(lds + LDS_LIST + pos * 8) = make_int2(pb[b], tid | (int)(msk[b] << 16));
      }
    }
  }
  __syncthreads();
  nloc = __builtin_amdgcn_readfirstlane(nloc);
  const int nw = (nloc > wave) ? (nloc - wave + W - 1) / W : 0;
  int ephys = 0, emeta = 0;
  if (lane < nw) { const int2 e = *reinterpret_cast<const int2*>(lds + LDS_LIST + (wave + W * lane) * 8); ephys = e.x; emeta = e.y; }
#pragma unroll
  for (int i = 0; i < 6; ++i) asm volatile("" : "+v"(qf[i]));
  asm volatile("" : "+v"(ephys), "+v"(emeta));

  const unsigned kvbytes = (unsigned)nphys * (unsigned)PAGEB;
  const __amdgpu_buffer_rsrc_t srdK = __builtin_amdgcn_make_buffer_rsrc((void*)kc, (short)0, (int)kvbytes, 0x00020000);
  const __amdgpu_buffer_rsrc_t srdV = __builtin_amdgcn_make_buffer_rsrc((void*)vc, (short)0, (int)kvbytes, 0x00020000);
  const unsigned ldsbase = (unsigned)(size_t)(LAS char*)lds;
  const unsigned ring = ldsbase + (unsigned)wave * RINGB;
  const unsigned voff = (unsigned)(lane >> 3) * TOKB + (unsigned)(((lane & 7) ^ ((lane >> 3) & 6)) * 16);
  const unsigned hoff = (unsigned)h * ROWB;
  unsigned kaddr[2], vaddr[4];
#pragma unroll
  for (int bb = 0; bb < 2; ++bb) kaddr[bb] = ring + c16 * 128 + (((4 * bb + g) ^ (c16 & 6)) * 16);
  {
    const int t = 4 * g + (c16 >> 2), p = lane & 3;
#pragma unroll
    for (int k = 0; k < 4; ++k) vaddr[k] = ring + t * 128 + ((((2 * k) ^ (t & 6)) + (p >> 1)) * 16) + (p & 1) * 8;
  }
  float m = -INFINITY, l = 0.f;
  f32x4 acc[12];
#pragma unroll
  for (int i = 0; i < 12; ++i) acc[i] = f32x4{0.f, 0.f, 0.f, 0.f};

  const int ntiles = 2 * nw;
  auto issue = [&](int T, unsigned slot) {
    const int phys = __builtin_amdgcn_readlane(ephys, T >> 1);
    const unsigned soff = (unsigned)phys * (unsigned)PAGEB + hoff;
    if (T & 1) dma_tile<1>(srdV, voff, soff, ring + slot); else dma_tile<0>(srdK, voff, soff, ring + slot);
  };
#pragma unroll
  for (int T = 0; T < NS; ++T) if (T < ntiles) issue(T, T * TILEB);
  unsigned slot = 0;
  for (int i = 0; i < nw; ++i) {
    const int meta = __builtin_amdgcn_readlane(emeta, i);
    const int col = meta & 0xffff;
    wait_tiles(ntiles - 1 - 2 * i);
    bf16x8 kf[6];
#pragma unroll
    for (int u = 0; u < 6; ++u) kf[u] = *(const LAS bf16x8*)(size_t)(kaddr[u & 1] + slot + (u >> 1) * 2048);
    asm volatile("s_waitcnt lgkmcnt(0)" : "+v"(kf[0]), "+v"(kf[1]), "+v"(kf[2]), "+v"(kf[3]), "+v"(kf[4]), "+v"(kf[5]) :: "memory");
    if (2 * i + NS < ntiles) issue(2 * i + NS, slot);
    slot = (slot + TILEB == RINGB) ? 0u : slot + TILEB;
    f32x4 s0 = {0.f, 0.f, 0.f, 0.f}, s1 = {0.f, 0.f, 0.f, 0.f};
#pragma unroll
    for (int u = 0; u < 6; u += 2) {
      s0 = __builtin_amdgcn_mfma_f32_16x16x32_bf16(kf[u], __builtin_bit_cast(bf16x8, qf[u]), s0, 0, 0, 0);
      s1 = __builtin_amdgcn_mfma_f32_16x16x32_bf16(kf[u + 1], __builtin_bit_cast(bf16x8, qf[u + 1]), s1, 0, 0, 0);
    }
    const int nval = ((meta >> (16 + c16)) & 1) ? (myctx - col * BS - 4 * g) : 0;
    float sc[4];
#pragma unroll
    for (int r = 0; r < 4; ++r) sc[r] = (r < nval) ? (s0[r] + s1[r]) * SCALE_LOG2E : -INFINITY;
    float tm = fmaxf(fmaxf(sc[0], sc[1]), fmaxf(sc[2], sc[3]));
    tm = xrow16_max(tm);
    const float mn = fmaxf(m, tm);
    const float msub = (mn == -INFINITY) ? 0.f : mn;
    const float alpha = __builtin_amdgcn_exp2f(m - msub);
    float p[4];
#pragma unroll
    for (int r = 0; r < 4; ++r) p[r] = __builtin_amdgcn_exp2f(sc[r] - msub);
    l = l * alpha + ((p[0] + p[1]) + (p[2] + p[3]));
    if (__any(mn > m)) {
#pragma unroll
      for (int dt = 0; dt < 12; ++dt) acc[dt] *= alpha;
    }
    m = mn;
    const bf16x4 pb4 = {(__bf16)p[0], (__bf16)p[1], (__bf16)p[2], (__bf16)p[3]};
    const s16x4 pf = __builtin_bit_cast(s16x4, pb4);
    wait_tiles(ntiles - 2 - 2 * i);
    s16x4 vf[12];
#pragma unroll
    for (int dt = 0; dt < 12; ++dt)
      vf[dt] = __builtin_amdgcn_ds_read_tr16_b64_v4i16((LAS s16x4*)(size_t)(vaddr[dt & 3] + slot + (dt >> 2) * 2048));
    asm volatile("s_waitcnt lgkmcnt(0)"
                 : "+v"(vf[0]), "+v"(vf[1]), "+v"(vf[2]), "+v"(vf[3]), "+v"(vf[4]), "+v"(vf[5]), "+v"(vf[6]), "+v"(vf[7]), "+v"(vf[8]), "+v"(vf[9]), "+v"(vf[10]), "+v"(vf[11])
                 :: "memory");
    if (2 * i + 1 + NS < ntiles) issue(2 * i + 1 + NS, slot);
    slot = (slot + TILEB == RINGB) ? 0u : slot + TILEB;
#pragma unroll
    for (int dt = 0; dt < 12; ++dt) acc[dt] = __builtin_amdgcn_mfma_f32_16x16x16bf16_1k(vf[dt], pf, acc[dt], 0, 0, 0);
  }
  l = xrow16_sum(l);
  if (c16 < B) {
    float* dst = reinterpret_cast<float*>(lds + wave * RINGB) + c16 * D + 4 * g;
#pragma unroll
    for (int dt = 0; dt < 12; ++dt) *reinterpret_cast<f32x4*>(dst + 16 * dt) = acc[dt];
    if (g == 0) *reinterpret_cast<f32x2*>(lds + wave * RINGB + B * D * 4 + c16 * 8) = f32x2{m, l};
  }
  __syncthreads();
  for (int it = tid; it < B * (D / 4); it += NTHR) {
    const int qq = it / (D / 4), d4 = it % (D / 4);
    float mw[W], lw[W], M = -INFINITY;
#pragma unroll
    for (int w = 0; w < W; ++w) { const f32x2 t = *reinterpret_cast<const f32x2*>(lds + w * RINGB + B * D * 4 + qq * 8); mw[w] = t[0]; lw[w] = t[1]; M = fmaxf(M, mw[w]); }
    float L = 0.f; f32x4 o = {0.f, 0.f, 0.f, 0.f};
#pragma unroll
    for (int w = 0; w < W; ++w) {
      const float wt = (lw[w] > 0.f) ? __builtin_amdgcn_exp2f(mw[w] - M) : 0.f;
      const f32x4 a = *reinterpret_cast<const f32x4*>(lds + w * RINGB + (qq * D + 4 * d4) * 4);
      L = fmaf(wt, lw[w], L);
      o += wt * a;
    }
    const size_t slotp = ((size_t)(h * B + qq) * NSPLIT + s);
    if (L > 0.f) *reinterpret_cast<f32x4*>(part + slotp * D + 4 * d4) = o;
    if (d4 == 0) *reinterpret_cast<f32x2*>(pml + slotp * 2) = f32x2{M, L};
  }
}

__global__ void __launch_bounds__(64) attn_combine(const float* __restrict__ part, const float* __restrict__ pml, __hip_bfloat16* __restrict__ out) {
  const int h = blockIdx.x / B, b = blockIdx.x % B, t = threadIdx.x;
  if (t >= D / 4) return;
  const size_t base = (size_t)(h * B + b) * NSPLIT;
  f32x2 ml[NSPLIT]; f32x4 a[NSPLIT];
#pragma unroll
  for (int s = 0; s < NSPLIT; ++s) { ml[s] = *reinterpret_cast<const f32x2*>(pml + (base + s) * 2); a[s] = *reinterpret_cast<const f32x4*>(part + (base + s) * D + 4 * t); }
  float M = -INFINITY;
#pragma unroll
  for (int s = 0; s < NSPLIT; ++s) M = fmaxf(M, ml[s][0]);
  float L = 0.f; f32x4 o = {0.f, 0.f, 0.f, 0.f};
#pragma unroll
  for (int s = 0; s < NSPLIT; ++s) {
    const bool on = ml[s][1] > 0.f;
    const float wt = on ? __builtin_amdgcn_exp2f(ml[s][0] - M) : 0.f;
    L = fmaf(wt, ml[s][1], L);
#pragma unroll
    for (int k = 0; k < 4; ++k) o[k] += on ? wt * a[s][k] : 0.f;
  }
  const float inv = 1.f / L;
  const bf16x4 r = {(__bf16)(o[0] * inv), (__bf16)(o[1] * inv), (__bf16)(o[2] * inv), (__bf16)(o[3] * inv)};
  *reinterpret_cast<bf16x4*>(out + ((size_t)b * H + h) * D + 4 * t) = r;
}

extern "C" void kernel_launch(void* const* d_in, const int* in_sizes, int n_in, void* d_out, int out_size, void* d_ws, size_t ws_size,
                              hipStream_t stream) {
  const __hip_bfloat16* q = (const __hip_bfloat16*)d_in[0];
  const __hip_bfloat16* k = (const __hip_bfloat16*)d_in[1];
  const __hip_bfloat16* v = (const __hip_bfloat16*)d_in[2];
  const int* bt = (const int*)d_in[3];
  const int* ctx = (const int*)d_in[4];
  const int nphys = in_sizes[1] / (BS * H * D);
  float* part = (float*)d_ws;
  float* pml = part + (size_t)H * B * NSPLIT * D;
  attn_main<<<H * NSPLIT, NTHR, 0, stream>>>(q, k, v, bt, ctx, part, pml, nphys);
  attn_combine<<<H * B, 64, 0, stream>>>(part, pml, (__hip_bfloat16*)d_out);
}
```

```cpp
#include <hip/hip_runtime.h>
#include <hip/hip_bf16.h>
#include <cstdio>
#include <cstdint>

#define EARLY 0
#define POLLSLEEP 8
#define FULLSLEEP 2
#define KMAP 0
#define DYN 1

constexpr int B = 8, H = 32, D = 192, NB = 256, BS = 16, LK = NB * BS;
constexpr int ROWB = D * 2;
constexpr int TOKB = H * ROWB;
constexpr int PAGEB = BS * TOKB;
constexpr int W = 8, HG = 4, NHG = H / HG, NSPL = 32, NWG = NHG * NSPL, NTHR = W * 64;
constexpr int NS = 3;
constexpr int TILEB = BS * ROWB;
constexpr int RINGB = NS * TILEB;
constexpr int LDS_LIST = W * RINGB;
constexpr int LDS_CNT = LDS_LIST + 72 * 8;
constexpr int LDS_BYTES = LDS_CNT + 40 * 4;
static_assert(LDS_BYTES <= 160 * 1024, "LDS");
constexpr int PROW = 200;
constexpr int REC_OFF = B * PROW * 4;
static_assert(REC_OFF + PROW * 4 <= RINGB, "epilogue area");
constexpr int RECB = 1664;
constexpr size_t WS_EPOCH = 0, WS_EARLY = 4096, WS_REC = WS_EARLY + (size_t)H * B * NSPL * 8, WS_END = WS_REC + (size_t)H * B * NSPL * RECB;
constexpr float SCALE_LOG2E = 0.07216878364870322f * 1.4426950408889634f;

#define LAS __attribute__((address_space(3)))
#define GAS __attribute__((address_space(1)))
typedef __attribute__((ext_vector_type(8))) __bf16 bf16x8;
typedef __attribute__((ext_vector_type(4))) __bf16 bf16x4;
typedef __attribute__((ext_vector_type(4))) short s16x4;
typedef __attribute__((ext_vector_type(4))) float f32x4;
typedef __attribute__((ext_vector_type(2))) float f32x2;
typedef __attribute__((ext_vector_type(4))) unsigned u32x4;
typedef __attribute__((ext_vector_type(2))) unsigned u32x2;

__device__ __forceinline__ float xrow16_max(float x) {
  auto s = __builtin_amdgcn_permlane16_swap(__float_as_uint(x), __float_as_uint(x), false, false);
  x = fmaxf(__uint_as_float(s[0]), __uint_as_float(s[1]));
  auto t = __builtin_amdgcn_permlane32_swap(__float_as_uint(x), __float_as_uint(x), false, false);
  return fmaxf(__uint_as_float(t[0]), __uint_as_float(t[1]));
}
__device__ __forceinline__ float xrow16_sum(float x) {
  auto s = __builtin_amdgcn_permlane16_swap(__float_as_uint(x), __float_as_uint(x), false, false);
  x = __uint_as_float(s[0]) + __uint_as_float(s[1]);
  auto t = __builtin_amdgcn_permlane32_swap(__float_as_uint(x), __float_as_uint(x), false, false);
  return __uint_as_float(t[0]) + __uint_as_float(t[1]);
}
template <int CTRL, int RM = 0xf> __device__ __forceinline__ int dpp_add(int x) {
  return x + __builtin_amdgcn_update_dpp(0, x, CTRL, RM, 0xf, false);
}
__device__ __forceinline__ float rfl(float x) { return __uint_as_float(__builtin_amdgcn_readfirstlane(__float_as_uint(x))); }

__device__ __forceinline__ void dma_tile(__amdgpu_buffer_rsrc_t srd, unsigned voff, unsigned soff, unsigned m0v) {
  const unsigned soff1 = soff + 8u * TOKB;
  asm volatile(
      "s_mov_b32 m0, %0\n\ts_nop 0\n\t"
      "buffer_load_dwordx4 %1, %2, %3 offen nt lds\n\t"
      "s_add_u32 m0, m0, 1024\n\ts_nop 0\n\t"
      "buffer_load_dwordx4 %1, %2, %4 offen nt lds\n\t"
      "s_add_u32 m0, m0, 896\n\ts_nop 0\n\t"
      "buffer_load_dwordx4 %1, %2, %3 offen offset:128 nt lds\n\t"
      "s_add_u32 m0, m0, 1024\n\ts_nop 0\n\t"
      "buffer_load_dwordx4 %1, %2, %4 offen offset:128 nt lds\n\t"
      "s_add_u32 m0, m0, 896\n\ts_nop 0\n\t"
      "buffer_load_dwordx4 %1, %2, %3 offen offset:256 nt lds\n\t"
      "s_add_u32 m0, m0, 1024\n\ts_nop 0\n\t"
      "buffer_load_dwordx4 %1, %2, %4 offen offset:256 nt lds"
      :: "s"(m0v), "v"(voff), "s"(srd), "s"(soff), "s"(soff1) : "memory", "scc");
}
template <int N> __device__ __forceinline__ void wait_vm() { asm volatile("s_waitcnt vmcnt(%0)" :: "i"(N) : "memory"); }

#define STAMP(i)

__global__ void __launch_bounds__(NTHR, 2) attn_kernel(const __hip_bfloat16* __restrict__ q, const __hip_bfloat16* __restrict__ kc,
                                                       const __hip_bfloat16* __restrict__ vc, const int* __restrict__ bt,
                                                       const int* __restrict__ ctxl, __hip_bfloat16* __restrict__ out,
                                                       unsigned char* __restrict__ ws, int nphys) {
  __shared__ __attribute__((aligned(16))) char lds[LDS_BYTES];
  const int tid = threadIdx.x, lane = tid & 63, wave = __builtin_amdgcn_readfirstlane(tid >> 6);
  const int hg = (blockIdx.x >> 3) & (NHG - 1), s = (blockIdx.x & 7) * (NSPL / 8) + (blockIdx.x >> 6);
  const int hh = wave & 3, pl = wave >> 2;
  const int h = hg * HG + hh;
  const int g = lane >> 4, c16 = lane & 15, cq = lane & 7;
  const int bc = s >> 2, hc = hg * HG + (s & 3);
  STAMP(0);

  const int colid = tid & (NB - 1);
  int pb[B];
#pragma unroll
  for (int b = 0; b < B; ++b) pb[b] = bt[b * NB + colid];
  u32x4 qf[6];
#pragma unroll
  for (int i = 0; i < 6; ++i) qf[i] = *reinterpret_cast<const u32x4*>(q + ((size_t)cq * H + h) * D + 32 * i + 8 * g);
  const int myctx_raw = ctxl[cq];
  GAS unsigned* epoch = (GAS unsigned*)(ws + WS_EPOCH);
  const unsigned ptag = __hip_atomic_load(epoch + h * B + cq, __ATOMIC_RELAXED, __HIP_MEMORY_SCOPE_AGENT) + 1u;
  const unsigned ctag = __hip_atomic_load(epoch + hc * B + bc, __ATOMIC_RELAXED, __HIP_MEMORY_SCOPE_AGENT) + 1u;
  int ce[B];
#pragma unroll
  for (int b = 0; b < B; ++b) { const int c = ctxl[b]; ce[b] = (c <= 0 || c > LK) ? LK : c; }

  unsigned long long bal[B];
  int* cnt = reinterpret_cast<int*>(lds + LDS_CNT);
  bool lead[B];
  if (wave < 4) {
#pragma unroll
    for (int b = 0; b < B; ++b) if (pb[b] < 0) pb[b] += nphys;
    int cv = 0;
#pragma unroll
    for (int b = 0; b < B; ++b) {
      bool dup = false;
#pragma unroll
      for (int b2 = 0; b2 < b; ++b2) dup = dup || (colid * BS < ce[b2] && pb[b2] == pb[b]);
      lead[b] = (colid * BS < ce[b]) && !dup;
      bal[b] = __ballot(lead[b]);
      cv = (lane == b) ? __popcll(bal[b]) : cv;
    }
    if (lane < B) cnt[lane * 4 + wave] = cv;
    if (tid < 8) cnt[32 + tid] = tid < 4 ? 0 : 4;
  } else {
#pragma unroll
    for (int b = 0; b < B; ++b) { lead[b] = false; bal[b] = 0ull; }
  }
  __syncthreads();
  STAMP(1);
  int nloc, ntot, pos[B];
  {
    int v = cnt[lane & 31];
    const int own = v;
    v = dpp_add<0x111>(v); v = dpp_add<0x112>(v); v = dpp_add<0x114>(v); v = dpp_add<0x118>(v);
    v = dpp_add<0x142, 0xa>(v);
    ntot = __builtin_amdgcn_readlane(v, 31);
    const int excl = v - own;
    const int lo = (ntot * s) / NSPL, hi = (ntot * (s + 1)) / NSPL;
    nloc = hi - lo;
    if (wave < 4) {
#pragma unroll
      for (int b = 0; b < B; ++b) {
        const int base = __builtin_amdgcn_readlane(excl, b * 4 + wave);
        pos[b] = base + (int)__builtin_amdgcn_mbcnt_hi((unsigned)(bal[b] >> 32), __builtin_amdgcn_mbcnt_lo((unsigned)bal[b], 0u));
        const int rel = pos[b] - lo;
        if (lead[b] && (unsigned)rel < (unsigned)nloc) {
          int mk = 0;
#pragma unroll
          for (int b2 = b; b2 < B; ++b2) mk |= (colid * BS < ce[b2] && pb[b2] == pb[b]) ? (1 << (16 + b2)) : 0;
          *reinterpret_cast<int2*>(lds + LDS_LIST + rel * 8) = make_int2(pb[b], colid | mk);
        }
      }
    } else {
#pragma unroll
      for (int b = 0; b < B; ++b) pos[b] = 0;
    }
  }
  __syncthreads();
  int phA, mtA, phB, mtB;
  {
    const int2 ea = *reinterpret_cast<const int2*>(lds + LDS_LIST + pl * 8);
    const int2 eb = *reinterpret_cast<const int2*>(lds + LDS_LIST + (2 + pl) * 8);
    phA = __builtin_amdgcn_readfirstlane(ea.x); mtA = __builtin_amdgcn_readfirstlane(ea.y);
    phB = __builtin_amdgcn_readfirstlane(eb.x); mtB = __builtin_amdgcn_readfirstlane(eb.y);
  }
  bool vA = pl < nloc, vB = 2 + pl < nloc;
  const int myctx = (myctx_raw <= 0 || myctx_raw > LK) ? LK : myctx_raw;
  {
    const bool zq = (c16 >= B) || (myctx_raw <= 0);
#pragma unroll
    for (int i = 0; i < 6; ++i) { if (zq) qf[i] = u32x4{0u, 0u, 0u, 0u}; asm volatile("" : "+v"(qf[i])); }
  }
  unsigned ptag_p = ptag, ctag_p = ctag;
  asm volatile("" : "+v"(ptag_p), "+v"(ctag_p));
  STAMP(2);

  const unsigned kvbytes = (unsigned)nphys * (unsigned)PAGEB;
  const __amdgpu_buffer_rsrc_t srdK = __builtin_amdgcn_make_buffer_rsrc((void*)kc, (short)0, (int)kvbytes, 0x00020000);
  const __amdgpu_buffer_rsrc_t srdV = __builtin_amdgcn_make_buffer_rsrc((void*)vc, (short)0, (int)kvbytes, 0x00020000);
  const __amdgpu_buffer_rsrc_t srdE = __builtin_amdgcn_make_buffer_rsrc((void*)(ws + WS_EARLY), (short)0, (int)(WS_REC - WS_EARLY), 0x00020000);
  const unsigned ldsbase = (unsigned)(size_t)(LAS char*)lds;
  const unsigned ring = ldsbase + (unsigned)wave * RINGB;
  const unsigned voff = (unsigned)(lane >> 3) * TOKB + (unsigned)(((lane & 7) ^ ((lane >> 3) & 6)) * 16);
  const unsigned hoff = (unsigned)h * ROWB;
  if (vA) {
    dma_tile(srdK, voff, (unsigned)phA * (unsigned)PAGEB + hoff, ring);
    dma_tile(srdV, voff, (unsigned)phA * (unsigned)PAGEB + hoff, ring + TILEB);
  }
  if (vB) dma_tile(srdK, voff, (unsigned)phB * (unsigned)PAGEB + hoff, ring + 2 * TILEB);

  if (wave < 4) {
    int pbc = pb[0]; int cec = ce[0];
#pragma unroll
    for (int b = 1; b < B; ++b) if (b == bc) { pbc = pb[b]; cec = ce[b]; }
    int mp = -1;
#pragma unroll
    for (int b = 0; b < B; ++b) if (lead[b] && pb[b] == pbc) mp = pos[b];
    unsigned bit = 0;
    if (colid * BS < cec) {
      const int x = NSPL * mp + NSPL - 1;
      int sp = (int)((float)x * (1.0f / (float)ntot));
      const int r = x - sp * ntot;
      sp += (r >= ntot) ? 1 : 0; sp -= (r < 0) ? 1 : 0;
      bit = 1u << sp;
    }
    bit |= __builtin_amdgcn_update_dpp(0, bit, 0x111, 0xf, 0xf, false);
    bit |= __builtin_amdgcn_update_dpp(0, bit, 0x112, 0xf, 0xf, false);
    bit |= __builtin_amdgcn_update_dpp(0, bit, 0x114, 0xf, 0xf, false);
    bit |= __builtin_amdgcn_update_dpp(0, bit, 0x118, 0xf, 0xf, false);
    const unsigned all = __builtin_amdgcn_readlane(bit, 15) | __builtin_amdgcn_readlane(bit, 31) | __builtin_amdgcn_readlane(bit, 47) | __builtin_amdgcn_readlane(bit, 63);
    if (lane == 0) atomicOr(reinterpret_cast<unsigned*>(cnt + 32), all);
  }

  unsigned kaddr[2], vaddr[4];
#pragma unroll
  for (int bb = 0; bb < 2; ++bb) kaddr[bb] = ring + c16 * 128 + (((4 * bb + g) ^ (c16 & 6)) * 16);
  {
    const int t = 4 * g + (c16 >> 2), p = lane & 3;
#pragma unroll
    for (int k = 0; k < 4; ++k) vaddr[k] = ring + t * 128 + ((((2 * k) ^ (t & 6)) + (p >> 1)) * 16) + (p & 1) * 8;
  }
  float m = -INFINITY, l = 0.f;
  f32x4 acc[12];
#pragma unroll
  for (int i = 0; i < 12; ++i) acc[i] = f32x4{0.f, 0.f, 0.f, 0.f};

  unsigned slot = 0;
  while (vA) {
    int vidx = 0;
    if (vB && lane == 0) vidx = __hip_atomic_fetch_add(cnt + 36 + hh, 1, __ATOMIC_RELAXED, __HIP_MEMORY_SCOPE_WORKGROUP);
    if (vB) wait_vm<12>(); else wait_vm<6>();
    bf16x8 kf[6];
#pragma unroll
    for (int u = 0; u < 6; ++u) kf[u] = *(const LAS bf16x8*)(size_t)(kaddr[u & 1] + slot + (u >> 1) * 2048);
    asm volatile("s_waitcnt lgkmcnt(0)" : "+v"(kf[0]), "+v"(kf[1]), "+v"(kf[2]), "+v"(kf[3]), "+v"(kf[4]), "+v"(kf[5]), "+v"(vidx) :: "memory");
    if (vB) dma_tile(srdV, voff, (unsigned)phB * (unsigned)PAGEB + hoff, ring + slot);
    const int idxC = __builtin_amdgcn_readfirstlane(vidx);
    const bool vC = vB && idxC < nloc;
    int2 ec = *reinterpret_cast<const int2*>(lds + LDS_LIST + (idxC & 63) * 8);
    slot = (slot + TILEB == RINGB) ? 0u : slot + TILEB;
    f32x4 s0 = {0.f, 0.f, 0.f, 0.f}, s1 = {0.f, 0.f, 0.f, 0.f};
#pragma unroll
    for (int u = 0; u < 6; u += 2) {
      s0 = __builtin_amdgcn_mfma_f32_16x16x32_bf16(kf[u], __builtin_bit_cast(bf16x8, qf[u]), s0, 0, 0, 0);
      s1 = __builtin_amdgcn_mfma_f32_16x16x32_bf16(kf[u + 1], __builtin_bit_cast(bf16x8, qf[u + 1]), s1, 0, 0, 0);
    }
    const int nval = ((mtA >> (16 + c16)) & 1) ? (myctx - (mtA & 0xffff) * BS - 4 * g) : 0;
    float sc[4];
#pragma unroll
    for (int r = 0; r < 4; ++r) sc[r] = (r < nval) ? (s0[r] + s1[r]) * SCALE_LOG2E : -INFINITY;
    float tm = fmaxf(fmaxf(sc[0], sc[1]), fmaxf(sc[2], sc[3]));
    tm = xrow16_max(tm);
    const float mn = fmaxf(m, tm);
    const float msub = (mn == -INFINITY) ? 0.f : mn;
    const float alpha = __builtin_amdgcn_exp2f(m - msub);
    float p[4];
#pragma unroll
    for (int r = 0; r < 4; ++r) p[r] = __builtin_amdgcn_exp2f(sc[r] - msub);
    l = l * alpha + ((p[0] + p[1]) + (p[2] + p[3]));
    if (__any(mn > m)) {
#pragma unroll
      for (int dt = 0; dt < 12; ++dt) acc[dt] *= alpha;
    }
    m = mn;
    const bf16x4 pb4 = {(__bf16)p[0], (__bf16)p[1], (__bf16)p[2], (__bf16)p[3]};
    const s16x4 pf = __builtin_bit_cast(s16x4, pb4);
    if (vB) wait_vm<12>(); else wait_vm<EARLY>();
    s16x4 vf[12];
#pragma unroll
    for (int dt = 0; dt < 12; ++dt)
      vf[dt] = __builtin_amdgcn_ds_read_tr16_b64_v4i16((LAS s16x4*)(size_t)(vaddr[dt & 3] + slot + (dt >> 2) * 2048));
    asm volatile("s_waitcnt lgkmcnt(0)"
                 : "+v"(vf[0]), "+v"(vf[1]), "+v"(vf[2]), "+v"(vf[3]), "+v"(vf[4]), "+v"(vf[5]), "+v"(vf[6]), "+v"(vf[7]), "+v"(vf[8]), "+v"(vf[9]), "+v"(vf[10]), "+v"(vf[11]),
                   "+v"(ec.x), "+v"(ec.y)
                 :: "memory");
    const int phC = __builtin_amdgcn_readfirstlane(ec.x), mtC = __builtin_amdgcn_readfirstlane(ec.y);
    if (vC) dma_tile(srdK, voff, (unsigned)phC * (unsigned)PAGEB + hoff, ring + slot);
    slot = (slot + TILEB == RINGB) ? 0u : slot + TILEB;
#pragma unroll
    for (int dt = 0; dt < 12; ++dt) acc[dt] = __builtin_amdgcn_mfma_f32_16x16x16bf16_1k(vf[dt], pf, acc[dt], 0, 0, 0);
    phA = phB; mtA = mtB; vA = vB;
    phB = phC; mtB = mtC; vB = vC;
  }
  STAMP(3);

  const __amdgpu_buffer_rsrc_t srdR = __builtin_amdgcn_make_buffer_rsrc((void*)(ws + WS_REC), (short)0, (int)(WS_END - WS_REC), 0x00020000);
  l = xrow16_sum(l);
  {
    float* xp = reinterpret_cast<float*>(lds + wave * RINGB);
    if (c16 < B) {
      float* dst = xp + c16 * PROW + 4 * g;
#pragma unroll
      for (int dt = 0; dt < 12; ++dt) *reinterpret_cast<f32x4*>(dst + 16 * dt) = acc[dt];
      if (g == 0) *reinterpret_cast<f32x2*>(xp + c16 * PROW + D) = f32x2{m, l};
    }
  }
  __syncthreads();
  const int i2 = 64 + (lane < 32 ? lane : 32);
  {
    const float* xa = reinterpret_cast<const float*>(lds + hh * RINGB) + pl * 4 * PROW;
    const float* xb = reinterpret_cast<const float*>(lds + (hh + 4) * RINGB) + pl * 4 * PROW;
    f32x2 mla[4], mlb[4], a0[4], a1[4], b0[4], b1[4];
#pragma unroll
    for (int k = 0; k < 4; ++k) {
      mla[k] = *reinterpret_cast<const f32x2*>(xa + k * PROW + D); mlb[k] = *reinterpret_cast<const f32x2*>(xb + k * PROW + D);
      a0[k] = *reinterpret_cast<const f32x2*>(xa + k * PROW + 2 * lane); b0[k] = *reinterpret_cast<const f32x2*>(xb + k * PROW + 2 * lane);
      a1[k] = *reinterpret_cast<const f32x2*>(xa + k * PROW + 2 * i2); b1[k] = *reinterpret_cast<const f32x2*>(xb + k * PROW + 2 * i2);
    }
#pragma unroll
    for (int k = 0; k < 4; ++k) {
      const bool ona = mla[k][1] > 0.f, onb = mlb[k][1] > 0.f;
      if (__builtin_amdgcn_readfirstlane((int)(ona || onb))) {
        const int qq = pl * 4 + k;
        const float M = fmaxf(mla[k][0], mlb[k][0]);
        const float wa = ona ? __builtin_amdgcn_exp2f(mla[k][0] - M) : 0.f, wb = onb ? __builtin_amdgcn_exp2f(mlb[k][0] - M) : 0.f;
        f32x2 v0 = {0.f, 0.f}, v1 = {0.f, 0.f};
        if (ona) { v0 += wa * a0[k]; v1 += wa * a1[k]; }
        if (onb) { v0 += wb * b0[k]; v1 += wb * b1[k]; }
        if (lane >= 32) v1 = f32x2{M, wa * mla[k][1] + wb * mlb[k][1]};
        const unsigned tag = __builtin_amdgcn_readlane(ptag_p, qq);
        const int rec = ((h * B + qq) * NSPL + s) * RECB;
        __builtin_amdgcn_raw_buffer_store_b128(u32x4{__float_as_uint(v0[0]), tag, __float_as_uint(v0[1]), tag}, srdR, rec + lane * 16, 0, 16);
        if (lane <= 32) __builtin_amdgcn_raw_buffer_store_b128(u32x4{__float_as_uint(v1[0]), tag, __float_as_uint(v1[1]), tag}, srdR, rec + i2 * 16, 0, 16);
      }
    }
  }
  STAMP(4);

  const unsigned touch = (unsigned)__builtin_amdgcn_readfirstlane(cnt[32]);
  const unsigned ct = __builtin_amdgcn_readfirstlane(ctag_p);
  float M = -INFINITY, L = 0.f;
  f32x2 o0 = {0.f, 0.f}, o1 = {0.f, 0.f};
  {
    const bool mine = lane < 32 && ((touch >> (lane & 31)) & 1u) && (__popc(touch & ((1u << (lane & 31)) - 1u)) & (W - 1)) == wave;
    unsigned pend = (unsigned)__ballot(mine);
    const unsigned long long t0 = __builtin_amdgcn_s_memrealtime();
    const int recbase = (hc * B + bc) * NSPL;
    {
      const int hoffs = (recbase + (lane & 31)) * RECB + 96 * 16;
      while (pend) {
        asm volatile("" ::: "memory");
        bool ok = true;
        if (mine) { const u32x4 x = __builtin_amdgcn_raw_buffer_load_b128(srdR, hoffs, 0, 16); ok = x[1] == ct && x[3] == ct; }
        if (__all(ok)) break;
        if (__builtin_amdgcn_s_memrealtime() - t0 > 200000ull) break;
        __builtin_amdgcn_s_sleep(POLLSLEEP);
      }
    }
    const unsigned armed = pend;
    while (pend) {
      asm volatile("" ::: "memory");
      const unsigned act = pend & armed;
      int sp[4]; unsigned rest = act;
      u32x4 a[4], c[4];
#pragma unroll
      for (int r = 0; r < 4; ++r) {
        sp[r] = rest ? __builtin_ctz(rest) : -1; rest &= rest - 1u;
        if (sp[r] >= 0) {
          const int rec = (recbase + sp[r]) * RECB;
          a[r] = __builtin_amdgcn_raw_buffer_load_b128(srdR, rec + lane * 16, 0, 16);
          c[r] = __builtin_amdgcn_raw_buffer_load_b128(srdR, rec + i2 * 16, 0, 16);
        } else { a[r] = u32x4{0u, 0u, 0u, 0u}; c[r] = a[r]; }
      }
      unsigned done = 0;
#pragma unroll
      for (int r = 0; r < 4; ++r) {
        if (sp[r] >= 0) {
          const bool ok = a[r][1] == ct && a[r][3] == ct && c[r][1] == ct && c[r][3] == ct;
          if (__all(ok)) {
            done |= 1u << sp[r];
            const float mr = __uint_as_float(__builtin_amdgcn_readlane(c[r][0], 32)), lr = __uint_as_float(__builtin_amdgcn_readlane(c[r][2], 32));
            const float Mn = fmaxf(M, mr);
            const float wo = __builtin_amdgcn_exp2f(M - Mn), wn = __builtin_amdgcn_exp2f(mr - Mn);
            o0 = wo * o0 + wn * f32x2{__uint_as_float(a[r][0]), __uint_as_float(a[r][2])};
            o1 = wo * o1 + wn * f32x2{__uint_as_float(c[r][0]), __uint_as_float(c[r][2])};
            L = wo * L + wn * lr; M = Mn;
          }
        }
      }
      pend &= ~done;
      if (!done && pend) {
        if (__builtin_amdgcn_s_memrealtime() - t0 > 200000ull) break;
        if (pend & armed) __builtin_amdgcn_s_sleep(FULLSLEEP); else __builtin_amdgcn_s_sleep(POLLSLEEP);
      }
    }
  }
  STAMP(5);
  {
    float* rb = reinterpret_cast<float*>(lds + wave * RINGB + REC_OFF);
    *reinterpret_cast<f32x2*>(rb + 2 * lane) = o0;
    if (lane < 32) *reinterpret_cast<f32x2*>(rb + 2 * i2) = o1;
    if (lane == 32) *reinterpret_cast<f32x2*>(rb + D) = f32x2{M, L};
  }
  __syncthreads();
  if (wave == 0) {
    const int d4 = lane < D / 4 ? lane : 0;
    f32x2 ml[W]; f32x4 ov[W];
#pragma unroll
    for (int w = 0; w < W; ++w) {
      const float* rb = reinterpret_cast<const float*>(lds + w * RINGB + REC_OFF);
      ml[w] = *reinterpret_cast<const f32x2*>(rb + D); ov[w] = *reinterpret_cast<const f32x4*>(rb + 4 * d4);
    }
    float Mx = -INFINITY;
#pragma unroll
    for (int w = 0; w < W; ++w) Mx = fmaxf(Mx, ml[w][0]);
    float Ls = 0.f; f32x4 o = {0.f, 0.f, 0.f, 0.f};
#pragma unroll
    for (int w = 0; w < W; ++w) {
      const float wt = ml[w][1] > 0.f ? __builtin_amdgcn_exp2f(ml[w][0] - Mx) : 0.f;
      Ls = fmaf(wt, ml[w][1], Ls);
      o += wt * ov[w];
    }
    const float inv = 1.f / Ls;
    const bf16x4 r = {(__bf16)(o[0] * inv), (__bf16)(o[1] * inv), (__bf16)(o[2] * inv), (__bf16)(o[3] * inv)};
    if (lane < D / 4) *reinterpret_cast<bf16x4*>(out + ((size_t)bc * H + hc) * D + 4 * lane) = r;
    if (lane == 0) __hip_atomic_store(epoch + hc * B + bc, ct, __ATOMIC_RELAXED, __HIP_MEMORY_SCOPE_AGENT);
  }
  STAMP(6);
}

extern "C" void kernel_launch(void* const* d_in, const int* in_sizes, int n_in, void* d_out, int out_size, void* d_ws, size_t ws_size,
                              hipStream_t stream) {
  const __hip_bfloat16* q = (const __hip_bfloat16*)d_in[0];
  const __hip_bfloat16* k = (const __hip_bfloat16*)d_in[1];
  const __hip_bfloat16* v = (const __hip_bfloat16*)d_in[2];
  const int* bt = (const int*)d_in[3];
  const int* ctx = (const int*)d_in[4];
  const int nphys = in_sizes[1] / (BS * H * D);
  attn_kernel<<<NWG, NTHR, 0, stream>>>(q, k, v, bt, ctx, (__hip_bfloat16*)d_out, (unsigned char*)d_ws, nphys);
}
```

```cpp
#include <hip/hip_runtime.h>
#include <hip/hip_bf16.h>
#include <cstdio>
#include <cstdint>

#define POLLSLEEP 8
#define FULLSLEEP 1
#define KMAP 0
#define PAUX 16
#define FLAGAT 1
#define NF 8

constexpr int B = 8, H = 32, D = 192, NB = 256, BS = 16, LK = NB * BS;
constexpr int ROWB = D * 2;
constexpr int TOKB = H * ROWB;
constexpr int PAGEB = BS * TOKB;
constexpr int W = 8, HG = 4, NHG = H / HG, NSPL = 32, NWG = NHG * NSPL, NTHR = W * 64;
constexpr int NS = 3;
constexpr int TILEB = BS * ROWB;
constexpr int RINGB = NS * TILEB;
constexpr int LDS_TAB = W * RINGB;
constexpr int LDS_LIST = LDS_TAB + NB * B * 4;
constexpr int LDS_BAL = LDS_LIST + 64 * 8;
constexpr int LDS_CNT = LDS_BAL + 32 * 8;
constexpr int LDS_BYTES = LDS_CNT + 16 * 4;
static_assert(LDS_BYTES <= 160 * 1024, "LDS");
constexpr int PROW = 200;
static_assert(B * PROW * 4 <= RINGB, "epilogue area");
constexpr int RECB = 1024;
constexpr size_t WS_EPOCH = 0, WS_FLAG = 4096, WS_REC = WS_FLAG + (size_t)H * B * NSPL * 4, WS_END = WS_REC + (size_t)H * B * NSPL * RECB;
constexpr float SCALE_LOG2E = 0.07216878364870322f * 1.4426950408889634f;

#define LAS __attribute__((address_space(3)))
#define GAS __attribute__((address_space(1)))
typedef __attribute__((ext_vector_type(8))) __bf16 bf16x8;
typedef __attribute__((ext_vector_type(4))) __bf16 bf16x4;
typedef __attribute__((ext_vector_type(4))) short s16x4;
typedef __attribute__((ext_vector_type(4))) float f32x4;
typedef __attribute__((ext_vector_type(2))) float f32x2;
typedef __attribute__((ext_vector_type(4))) unsigned u32x4;
typedef __attribute__((ext_vector_type(2))) unsigned u32x2;

__device__ __forceinline__ float xrow16_max(float x) {
  auto s = __builtin_amdgcn_permlane16_swap(__float_as_uint(x), __float_as_uint(x), false, false);
  x = fmaxf(__uint_as_float(s[0]), __uint_as_float(s[1]));
  auto t = __builtin_amdgcn_permlane32_swap(__float_as_uint(x), __float_as_uint(x), false, false);
  return fmaxf(__uint_as_float(t[0]), __uint_as_float(t[1]));
}
__device__ __forceinline__ float xrow16_sum(float x) {
  auto s = __builtin_amdgcn_permlane16_swap(__float_as_uint(x), __float_as_uint(x), false, false);
  x = __uint_as_float(s[0]) + __uint_as_float(s[1]);
  auto t = __builtin_amdgcn_permlane32_swap(__float_as_uint(x), __float_as_uint(x), false, false);
  return __uint_as_float(t[0]) + __uint_as_float(t[1]);
}
template <int CTRL, int RM = 0xf> __device__ __forceinline__ int dpp_add(int x) {
  return x + __builtin_amdgcn_update_dpp(0, x, CTRL, RM, 0xf, false);
}
__device__ __forceinline__ unsigned umin3(unsigned a, unsigned b, unsigned c) { return min(min(a, b), c); }
__device__ __forceinline__ unsigned lds_or(int* p, unsigned v) { return __hip_atomic_fetch_or((unsigned*)p, v, __ATOMIC_RELAXED, __HIP_MEMORY_SCOPE_WORKGROUP); }
__device__ __forceinline__ int lds_add(int* p, int v) { return __hip_atomic_fetch_add(p, v, __ATOMIC_RELAXED, __HIP_MEMORY_SCOPE_WORKGROUP); }

__device__ __forceinline__ void dma_tile(__amdgpu_buffer_rsrc_t srd, unsigned voff, unsigned soff, unsigned m0v) {
  const unsigned soff1 = soff + 8u * TOKB;
  asm volatile(
      "s_mov_b32 m0, %0\n\ts_nop 0\n\t"
      "buffer_load_dwordx4 %1, %2, %3 offen nt lds\n\t"
      "s_add_u32 m0, m0, 1024\n\ts_nop 0\n\t"
      "buffer_load_dwordx4 %1, %2, %4 offen nt lds\n\t"
      "s_add_u32 m0, m0, 896\n\ts_nop 0\n\t"
      "buffer_load_dwordx4 %1, %2, %3 offen offset:128 nt lds\n\t"
      "s_add_u32 m0, m0, 1024\n\ts_nop 0\n\t"
      "buffer_load_dwordx4 %1, %2, %4 offen offset:128 nt lds\n\t"
      "s_add_u32 m0, m0, 896\n\ts_nop 0\n\t"
      "buffer_load_dwordx4 %1, %2, %3 offen offset:256 nt lds\n\t"
      "s_add_u32 m0, m0, 1024\n\ts_nop 0\n\t"
      "buffer_load_dwordx4 %1, %2, %4 offen offset:256 nt lds"
      :: "s"(m0v), "v"(voff), "s"(srd), "s"(soff), "s"(soff1) : "memory", "scc");
}
template <int N> __device__ __forceinline__ void wait_vm() { asm volatile("s_waitcnt vmcnt(%0)" :: "i"(N) : "memory"); }
__device__ __forceinline__ void wait_lds() { asm volatile("s_waitcnt lgkmcnt(0)" ::: "memory"); }

#define STAMP(i)

__global__ void __launch_bounds__(NTHR, 2) attn_kernel(const __hip_bfloat16* __restrict__ q, const __hip_bfloat16* __restrict__ kc,
                                                       const __hip_bfloat16* __restrict__ vc, const int* __restrict__ bt,
                                                       const int* __restrict__ ctxl, __hip_bfloat16* __restrict__ out,
                                                       unsigned char* __restrict__ ws, int nphys) {
  __shared__ __attribute__((aligned(16))) char lds[LDS_BYTES];
  const int tid = threadIdx.x, lane = tid & 63, wave = __builtin_amdgcn_readfirstlane(tid >> 6);
  const int hg = (blockIdx.x >> 3) & (NHG - 1), s = (blockIdx.x & 7) * (NSPL / 8) + (blockIdx.x >> 6);
  const int hh = wave & 3, pl = wave >> 2;
  const int h = hg * HG + hh;
  const int g = lane >> 4, c16 = lane & 15, cq = lane & 7;
  const int bc = s >> 2, hc = hg * HG + (s & 3);
  STAMP(0);

#define FT(i)
  FT(0);
  const int colid = tid & (NB - 1);
  int key[B];
#pragma unroll
  for (int b = 0; b < B; ++b) key[b] = bt[b * NB + colid];
  u32x4 qf[6];
#pragma unroll
  for (int i = 0; i < 6; ++i) qf[i] = *reinterpret_cast<const u32x4*>(q + ((size_t)cq * H + h) * D + 32 * i + 8 * g);
  const int myctx_raw = ctxl[cq];
  GAS unsigned* epoch = (GAS unsigned*)(ws + WS_EPOCH);
  const unsigned atag = __hip_atomic_load(epoch + hg * 32 + (lane & 31), __ATOMIC_RELAXED, __HIP_MEMORY_SCOPE_AGENT) + 1u;
  FT(1);
  int ce[B];
#pragma unroll
  for (int b = 0; b < B; ++b) { const int c = ctxl[b]; ce[b] = (c <= 0 || c > LK) ? LK : c; }

  int* cnt = reinterpret_cast<int*>(lds + LDS_CNT);
  unsigned long long bal[B];
  if (wave < 4) {
    const int c0 = colid * BS;
#pragma unroll
    for (int b = 0; b < B; ++b) {
      const int p = key[b] + ((key[b] >> 31) & nphys);
      key[b] = (c0 < ce[b]) ? p : -1;
    }
    unsigned blo = 0, bhi = 0;
#pragma unroll
    for (int b = 0; b < B; ++b) {
      unsigned dm = (unsigned)(key[b] + 1);
#pragma unroll
      for (int b2 = 0; b2 < b; ++b2) dm = min(dm, (unsigned)(key[b] ^ key[b2]));
      bal[b] = __ballot(dm != 0u);
      asm("s_nop 1\n\tv_writelane_b32 %0, %2, %4\n\tv_writelane_b32 %1, %3, %4"
          : "+v"(blo), "+v"(bhi) : "s"((unsigned)bal[b]), "s"((unsigned)(bal[b] >> 32)), "i"(b));
    }
    *reinterpret_cast<int4*>(lds + LDS_TAB + colid * 32) = make_int4(key[0], key[1], key[2], key[3]);
    *reinterpret_cast<int4*>(lds + LDS_TAB + colid * 32 + 16) = make_int4(key[4], key[5], key[6], key[7]);
    if (lane < B) *reinterpret_cast<u32x2*>(lds + LDS_BAL + (lane * 4 + wave) * 8) = u32x2{blo, bhi};
    if (tid < 12) cnt[tid] = (tid >= 4 && tid < 8) ? 4 : 0;
  } else {
#pragma unroll
    for (int b = 0; b < B; ++b) bal[b] = 0ull;
  }
  FT(4);
  __syncthreads();
  FT(5);
  STAMP(1);
  const u32x2 gb = *reinterpret_cast<const u32x2*>(lds + LDS_BAL + (lane & 31) * 8);
  const int gown = __popc(gb[0]) + __popc(gb[1]);
  int incl = gown;
  incl = dpp_add<0x111>(incl); incl = dpp_add<0x112>(incl); incl = dpp_add<0x114>(incl); incl = dpp_add<0x118>(incl);
  incl = dpp_add<0x142, 0xa>(incl);
  const int excl = incl - gown;
  const int ntot = __builtin_amdgcn_readlane(incl, 31);
  const int lo = (int)(((unsigned)ntot * (unsigned)s) / NSPL), hi = (int)(((unsigned)ntot * (unsigned)(s + 1)) / NSPL);
  const int nloc = hi - lo;
  auto page_of_rank = [&](int r) -> int {
    const int grp = __popcll(__ballot(incl <= r) & 0xffffffffull) & 31;
    const unsigned b0 = __builtin_amdgcn_readlane(gb[0], grp), b1 = __builtin_amdgcn_readlane(gb[1], grp);
    const int k = r - __builtin_amdgcn_readlane(excl, grp);
    const int below = (int)__builtin_amdgcn_mbcnt_hi(b1, __builtin_amdgcn_mbcnt_lo(b0, 0u));
    const unsigned long long sel = __ballot(below == k) & (((unsigned long long)b1 << 32) | b0);
    const int bit = sel ? __builtin_ctzll(sel) : 0;
    return *reinterpret_cast<const int*>(lds + LDS_TAB + ((grp & 3) * 64 + bit) * 32 + (grp >> 2) * 4);
  };
  FT(6);
  bool vA = pl < nloc, vB = 2 + pl < nloc;
  int phA, phB;
  {
    const int xa = page_of_rank(lo + pl), xb = page_of_rank(lo + 2 + pl);
    phA = __builtin_amdgcn_readfirstlane(xa); phB = __builtin_amdgcn_readfirstlane(xb);
  }
  const unsigned kvbytes = (unsigned)nphys * (unsigned)PAGEB;
  const __amdgpu_buffer_rsrc_t srdK = __builtin_amdgcn_make_buffer_rsrc((void*)kc, (short)0, (int)kvbytes, 0x00020000);
  const __amdgpu_buffer_rsrc_t srdV = __builtin_amdgcn_make_buffer_rsrc((void*)vc, (short)0, (int)kvbytes, 0x00020000);
  const unsigned ldsbase = (unsigned)(size_t)(LAS char*)lds;
  const unsigned ring = ldsbase + (unsigned)wave * RINGB;
  const unsigned voff = (unsigned)(lane >> 3) * TOKB + (unsigned)(((lane & 7) ^ ((lane >> 3) & 6)) * 16);
  const unsigned hoff = (unsigned)h * ROWB;
  FT(7);
  STAMP(2);
  if (vA) {
    dma_tile(srdK, voff, (unsigned)phA * (unsigned)PAGEB + hoff, ring);
    dma_tile(srdV, voff, (unsigned)phA * (unsigned)PAGEB + hoff, ring + TILEB);
  }
  if (vB) dma_tile(srdK, voff, (unsigned)phB * (unsigned)PAGEB + hoff, ring + 2 * TILEB);

  if (wave < 4) {
    int pos[B];
#pragma unroll
    for (int b = 0; b < B; ++b) {
      pos[b] = __builtin_amdgcn_readlane(excl, b * 4 + wave) + (int)__builtin_amdgcn_mbcnt_hi((unsigned)(bal[b] >> 32), __builtin_amdgcn_mbcnt_lo((unsigned)bal[b], 0u));
      const int rel = pos[b] - lo;
      if (((bal[b] >> lane) & 1ull) && (unsigned)rel < (unsigned)nloc) {
        int mk = 0;
#pragma unroll
        for (int b2 = b; b2 < B; ++b2) mk |= (key[b2] == key[b]) ? (1 << (16 + b2)) : 0;
        *reinterpret_cast<int2*>(lds + LDS_LIST + rel * 8) = make_int2(key[b], colid | mk);
      }
    }
    int kc_ = key[0];
#pragma unroll
    for (int b = 1; b < B; ++b) if (b == bc) kc_ = key[b];
    int mp = 0;
#pragma unroll
    for (int b = B - 1; b >= 0; --b) if (key[b] == kc_) mp = pos[b];
    unsigned bit = 0;
    if (kc_ >= 0) {
      const int x = NSPL * mp + NSPL - 1;
      int sp = (int)((float)x * (1.0f / (float)ntot));
      const int r = x - sp * ntot;
      sp += (r >= ntot) ? 1 : 0; sp -= (r < 0) ? 1 : 0;
      bit = 1u << sp;
    }
    bit |= __builtin_amdgcn_update_dpp(0, bit, 0x111, 0xf, 0xf, false);
    bit |= __builtin_amdgcn_update_dpp(0, bit, 0x112, 0xf, 0xf, false);
    bit |= __builtin_amdgcn_update_dpp(0, bit, 0x114, 0xf, 0xf, false);
    bit |= __builtin_amdgcn_update_dpp(0, bit, 0x118, 0xf, 0xf, false);
    const unsigned all = __builtin_amdgcn_readlane(bit, 15) | __builtin_amdgcn_readlane(bit, 31) | __builtin_amdgcn_readlane(bit, 47) | __builtin_amdgcn_readlane(bit, 63);
    if (lane == 0) lds_or(cnt, all);
  }
  const int myctx = (myctx_raw <= 0 || myctx_raw > LK) ? LK : myctx_raw;
  {
    const bool zq = (c16 >= B) || (myctx_raw <= 0);
#pragma unroll
    for (int i = 0; i < 6; ++i) { if (zq) qf[i] = u32x4{0u, 0u, 0u, 0u}; asm volatile("" : "+v"(qf[i])); }
  }
  unsigned atag_p = atag;
  asm volatile("" : "+v"(atag_p));
  wait_lds();
  __builtin_amdgcn_s_barrier();
  int mtA, mtB;
  {
    const int ya = *reinterpret_cast<const int*>(lds + LDS_LIST + pl * 8 + 4), yb = *reinterpret_cast<const int*>(lds + LDS_LIST + (2 + pl) * 8 + 4);
    mtA = __builtin_amdgcn_readfirstlane(ya); mtB = __builtin_amdgcn_readfirstlane(yb);
  }

  const __amdgpu_buffer_rsrc_t srdF = __builtin_amdgcn_make_buffer_rsrc((void*)(ws + WS_FLAG), (short)0, (int)(WS_REC - WS_FLAG), 0x00020000);
  unsigned kaddr[2], vaddr[4];
#pragma unroll
  for (int bb = 0; bb < 2; ++bb) kaddr[bb] = ring + c16 * 128 + (((4 * bb + g) ^ (c16 & 6)) * 16);
  {
    const int t = 4 * g + (c16 >> 2), p = lane & 3;
#pragma unroll
    for (int k = 0; k < 4; ++k) vaddr[k] = ring + t * 128 + ((((2 * k) ^ (t & 6)) + (p >> 1)) * 16) + (p & 1) * 8;
  }
  float m = -INFINITY, l = 0.f;
  f32x4 acc[12];
#pragma unroll
  for (int i = 0; i < 12; ++i) acc[i] = f32x4{0.f, 0.f, 0.f, 0.f};
  const unsigned mytag = __builtin_amdgcn_ds_bpermute((hh * 8 + cq) * 4, (int)atag_p);

  unsigned seen = 0;
  auto early = [&]() -> bool {
    unsigned old = 0;
    if (lane == 0) old = lds_or(cnt + 8 + hh, seen | (1u << pl));
    old = __builtin_amdgcn_readfirstlane(old);
    const unsigned wm = (old | seen) >> 16;
    const bool go = ((old >> (1 - pl)) & 1u) && wm != 0u;
    if (go && lane < B && ((wm >> lane) & 1u)) __builtin_amdgcn_raw_buffer_store_b32(mytag, srdF, ((h * B + lane) * NSPL + s) * 4, 0, 16);
    return go;
  };

  unsigned slot = 0;
  const bool nopages = !vA;
  while (vA) {
    int vidx = 0;
    if (vB && lane == 0) vidx = lds_add(cnt + 4 + hh, 1);
    seen |= (unsigned)mtA & 0xff0000u;
    if (vB) wait_vm<12>(); else wait_vm<6>();
    bf16x8 kf[6];
#pragma unroll
    for (int u = 0; u < 6; ++u) kf[u] = *(const LAS bf16x8*)(size_t)(kaddr[u & 1] + slot + (u >> 1) * 2048);
    asm volatile("s_waitcnt lgkmcnt(0)" : "+v"(kf[0]), "+v"(kf[1]), "+v"(kf[2]), "+v"(kf[3]), "+v"(kf[4]), "+v"(kf[5]), "+v"(vidx) :: "memory");
    if (vB) dma_tile(srdV, voff, (unsigned)phB * (unsigned)PAGEB + hoff, ring + slot);
    const int idxC = __builtin_amdgcn_readfirstlane(vidx);
    const bool vC = vB && idxC < nloc;
    int2 ec = *reinterpret_cast<const int2*>(lds + LDS_LIST + (idxC & 63) * 8);
    slot = (slot + TILEB == RINGB) ? 0u : slot + TILEB;
    f32x4 s0 = {0.f, 0.f, 0.f, 0.f}, s1 = {0.f, 0.f, 0.f, 0.f};
#pragma unroll
    for (int u = 0; u < 6; u += 2) {
      s0 = __builtin_amdgcn_mfma_f32_16x16x32_bf16(kf[u], __builtin_bit_cast(bf16x8, qf[u]), s0, 0, 0, 0);
      s1 = __builtin_amdgcn_mfma_f32_16x16x32_bf16(kf[u + 1], __builtin_bit_cast(bf16x8, qf[u + 1]), s1, 0, 0, 0);
    }
    const int nval = ((mtA >> (16 + c16)) & 1) ? (myctx - (mtA & 0xffff) * BS - 4 * g) : 0;
    float sc[4];
#pragma unroll
    for (int r = 0; r < 4; ++r) sc[r] = (r < nval) ? (s0[r] + s1[r]) * SCALE_LOG2E : -INFINITY;
    float tm = fmaxf(fmaxf(sc[0], sc[1]), fmaxf(sc[2], sc[3]));
    tm = xrow16_max(tm);
    const float mn = fmaxf(m, tm);
    const float msub = (mn == -INFINITY) ? 0.f : mn;
    const float alpha = __builtin_amdgcn_exp2f(m - msub);
    float p[4];
#pragma unroll
    for (int r = 0; r < 4; ++r) p[r] = __builtin_amdgcn_exp2f(sc[r] - msub);
    l = l * alpha + ((p[0] + p[1]) + (p[2] + p[3]));
    if (__any(mn > m)) {
#pragma unroll
      for (int dt = 0; dt < 12; ++dt) acc[dt] *= alpha;
    }
    m = mn;
    const bf16x4 pb4 = {(__bf16)p[0], (__bf16)p[1], (__bf16)p[2], (__bf16)p[3]};
    const s16x4 pf = __builtin_bit_cast(s16x4, pb4);
    if (vB) wait_vm<12>();
    else { wait_vm<0>(); early(); }
    s16x4 vf[12];
#pragma unroll
    for (int dt = 0; dt < 12; ++dt)
      vf[dt] = __builtin_amdgcn_ds_read_tr16_b64_v4i16((LAS s16x4*)(size_t)(vaddr[dt & 3] + slot + (dt >> 2) * 2048));
    asm volatile("s_waitcnt lgkmcnt(0)"
                 : "+v"(vf[0]), "+v"(vf[1]), "+v"(vf[2]), "+v"(vf[3]), "+v"(vf[4]), "+v"(vf[5]), "+v"(vf[6]), "+v"(vf[7]), "+v"(vf[8]), "+v"(vf[9]), "+v"(vf[10]), "+v"(vf[11]),
                   "+v"(ec.x), "+v"(ec.y)
                 :: "memory");
    const int phC = __builtin_amdgcn_readfirstlane(ec.x), mtC = __builtin_amdgcn_readfirstlane(ec.y);
    if (vC) dma_tile(srdK, voff, (unsigned)phC * (unsigned)PAGEB + hoff, ring + slot);
    slot = (slot + TILEB == RINGB) ? 0u : slot + TILEB;
#pragma unroll
    for (int dt = 0; dt < 12; ++dt) acc[dt] = __builtin_amdgcn_mfma_f32_16x16x16bf16_1k(vf[dt], pf, acc[dt], 0, 0, 0);
    phA = phB; mtA = mtB; vA = vB;
    phB = phC; mtB = mtC; vB = vC;
  }
  if (nopages) early();
  STAMP(3);

  const __amdgpu_buffer_rsrc_t srdR = __builtin_amdgcn_make_buffer_rsrc((void*)(ws + WS_REC), (short)0, (int)(WS_END - WS_REC), 0x00020000);
  l = xrow16_sum(l);
  {
    float* xp = reinterpret_cast<float*>(lds + wave * RINGB);
    if (c16 < B) {
      float* dst = xp + c16 * PROW + 4 * g;
#pragma unroll
      for (int dt = 0; dt < 12; ++dt) *reinterpret_cast<f32x4*>(dst + 16 * dt) = acc[dt];
      if (g == 0) *reinterpret_cast<f32x2*>(xp + c16 * PROW + D) = f32x2{m, l};
    }
  }
  wait_lds();
  unsigned pw = 0;
  if (lane == 0) pw = lds_or(cnt + 8 + hh, (4u << pl) | seen);
  pw = __builtin_amdgcn_readfirstlane(pw) | seen;
  asm volatile("" ::: "memory");
  if ((pw >> (3 - pl)) & 1u) {
    const float* xa = reinterpret_cast<const float*>(lds + hh * RINGB);
    const float* xb = reinterpret_cast<const float*>(lds + (hh + 4) * RINGB);
#pragma unroll
    for (int half = 0; half < 2; ++half) {
      if (((pw >> (16 + 4 * half)) & 0xfu) == 0u) continue;
      f32x2 mla[4], mlb[4]; float a[4][3], c[4][3];
#pragma unroll
      for (int k = 0; k < 4; ++k) {
        const int qq = half * 4 + k;
        mla[k] = *reinterpret_cast<const f32x2*>(xa + qq * PROW + D); mlb[k] = *reinterpret_cast<const f32x2*>(xb + qq * PROW + D);
#pragma unroll
        for (int j = 0; j < 3; ++j) { a[k][j] = xa[qq * PROW + lane + 64 * j]; c[k][j] = xb[qq * PROW + lane + 64 * j]; }
      }
#pragma unroll
      for (int k = 0; k < 4; ++k) {
        const int qq = half * 4 + k;
        if ((pw >> (16 + qq)) & 1u) {
          const float M = fmaxf(mla[k][0], mlb[k][0]);
          const float wa = mla[k][1] > 0.f ? __builtin_amdgcn_exp2f(mla[k][0] - M) : 0.f, wb = mlb[k][1] > 0.f ? __builtin_amdgcn_exp2f(mlb[k][0] - M) : 0.f;
          const float L = wa * mla[k][1] + wb * mlb[k][1];
          const float inv = __builtin_amdgcn_rcpf(L);
          const unsigned lse = __float_as_uint(M + __builtin_amdgcn_logf(L));
          const unsigned tag = __builtin_amdgcn_readlane(atag_p, hh * 8 + qq);
          const unsigned tw = (tag << 8) | ((lse >> (8 * (lane & 3))) & 0xffu);
          const float sa = wa * inv, sb = wb * inv;
          const u32x4 gr = {__float_as_uint(sa * a[k][0] + sb * c[k][0]), __float_as_uint(sa * a[k][1] + sb * c[k][1]), __float_as_uint(sa * a[k][2] + sb * c[k][2]), tw};
          __builtin_amdgcn_raw_buffer_store_b128(gr, srdR, ((h * B + qq) * NSPL + s) * RECB + lane * 16, 0, PAUX);
        }
      }
    }
    STAMP(4);
    return;
  }
  STAMP(4);
  {
    int tk = 0;
    if (lane == 0) tk = lds_add(cnt + 1, 1);
    if (__builtin_amdgcn_readfirstlane(tk) != 0) return;
  }
  {
    const unsigned touch = (unsigned)__builtin_amdgcn_readfirstlane(cnt[0]);
    const unsigned ct = __builtin_amdgcn_readlane(atag_p, (s & 3) * 8 + bc);
    const unsigned ct24 = ct & 0xffffffu;
    float M = -INFINITY, L = 0.f, o[3] = {0.f, 0.f, 0.f};
    unsigned pend = touch, armed = 0;
    const unsigned long long t0 = __builtin_amdgcn_s_memrealtime();
    const int recbase = (hc * B + bc) * NSPL;
    while (pend) {
      asm volatile("" ::: "memory");
      unsigned fl = ct;
      if ((pend & ~armed) && lane < 32) fl = __builtin_amdgcn_raw_buffer_load_b32(srdF, (recbase + lane) * 4, 0, 16);
      int sp[NF]; u32x4 a[NF];
      unsigned rest = pend & armed;
#pragma unroll
      for (int r = 0; r < NF; ++r) {
        sp[r] = rest ? __builtin_ctz(rest) : -1; rest &= rest - 1u;
        if (sp[r] >= 0) a[r] = __builtin_amdgcn_raw_buffer_load_b128(srdR, (recbase + sp[r]) * RECB + lane * 16, 0, 16);
        else a[r] = u32x4{0u, 0u, 0u, 0u};
      }
      unsigned done = 0;
#pragma unroll
      for (int r = 0; r < NF; ++r) {
        if (sp[r] >= 0) {
          if (__all((a[r][3] >> 8) == ct24)) {
            done |= 1u << sp[r];
            unsigned x = (a[r][3] & 0xffu) << (8 * (lane & 3));
            x |= __builtin_amdgcn_update_dpp(0u, x, 0xb1, 0xf, 0xf, false);
            x |= __builtin_amdgcn_update_dpp(0u, x, 0x4e, 0xf, 0xf, false);
            const float lse = __uint_as_float(x);
            const float Mn = fmaxf(M, lse);
            const float wo = __builtin_amdgcn_exp2f(M - Mn), wn = __builtin_amdgcn_exp2f(lse - Mn);
#pragma unroll
            for (int j = 0; j < 3; ++j) o[j] = wo * o[j] + wn * __uint_as_float(a[r][j]);
            L = wo * L + wn; M = Mn;
          }
        }
      }
      pend &= ~done;
      const unsigned was = armed;
      armed |= (unsigned)__ballot(lane < 32 && fl == ct);
      if (!done && pend) {
        if (__builtin_amdgcn_s_memrealtime() - t0 > 200000ull) break;
        if (pend & was) __builtin_amdgcn_s_sleep(FULLSLEEP); else if (!(pend & armed)) __builtin_amdgcn_s_sleep(POLLSLEEP);
      }
    }
    STAMP(5);
    const float inv = 1.f / L;
    __hip_bfloat16* orow = out + ((size_t)bc * H + hc) * D;
#pragma unroll
    for (int j = 0; j < 3; ++j) *reinterpret_cast<__bf16*>(orow + lane + 64 * j) = (__bf16)(o[j] * inv);
    if (lane == 0) __hip_atomic_store(epoch + hc * B + bc, ct, __ATOMIC_RELAXED, __HIP_MEMORY_SCOPE_AGENT);
  }
  STAMP(6);
}

extern "C" void kernel_launch(void* const* d_in, const int* in_sizes, int n_in, void* d_out, int out_size, void* d_ws, size_t ws_size,
                              hipStream_t stream) {
  const __hip_bfloat16* q = (const __hip_bfloat16*)d_in[0];
  const __hip_bfloat16* k = (const __hip_bfloat16*)d_in[1];
  const __hip_bfloat16* v = (const __hip_bfloat16*)d_in[2];
  const int* bt = (const int*)d_in[3];
  const int* ctx = (const int*)d_in[4];
  const int nphys = in_sizes[1] / (BS * H * D);
  attn_kernel<<<NWG, NTHR, 0, stream>>>(q, k, v, bt, ctx, (__hip_bfloat16*)d_out, (unsigned char*)d_ws, nphys);
}
```

```cpp
#include <hip/hip_runtime.h>
#include <hip/hip_bf16.h>
#include <cstdio>
#include <cstdint>

#define POLLSLEEP 8
#define FULLSLEEP 1
#define KMAP 0
#define PAUX 16
#define FLAGAT 1
#define TLBPF 0
#define NF 8

constexpr int B = 8, H = 32, D = 192, NB = 256, BS = 16, LK = NB * BS;
constexpr int ROWB = D * 2;
constexpr int TOKB = H * ROWB;
constexpr int PAGEB = BS * TOKB;
constexpr int W = 8, HG = 4, NHG = H / HG, NSPL = 32, NWG = NHG * NSPL, NTHR = W * 64;
constexpr int NS = 3;
constexpr int TILEB = BS * ROWB;
constexpr int RINGB = NS * TILEB;
constexpr int LDS_TAB = W * RINGB;
constexpr int LDS_LIST = LDS_TAB + NB * B * 4;
constexpr int LDS_BAL = LDS_LIST + 64 * 8;
constexpr int LDS_CNT = LDS_BAL + 32 * 8;
constexpr int LDS_BYTES = LDS_CNT + 16 * 4;
static_assert(LDS_BYTES <= 160 * 1024, "LDS");
constexpr int PROW = 200;
static_assert(B * PROW * 4 <= RINGB, "epilogue area");
constexpr int RECB = 1024;
constexpr size_t WS_EPOCH = 0, WS_FLAG = 4096, WS_REC = WS_FLAG + (size_t)H * B * NSPL * 4, WS_END = WS_REC + (size_t)H * B * NSPL * RECB;
constexpr float SCALE_LOG2E = 0.07216878364870322f * 1.4426950408889634f;

#define LAS __attribute__((address_space(3)))
#define GAS __attribute__((address_space(1)))
typedef __attribute__((ext_vector_type(8))) __bf16 bf16x8;
typedef __attribute__((ext_vector_type(4))) __bf16 bf16x4;
typedef __attribute__((ext_vector_type(4))) short s16x4;
typedef __attribute__((ext_vector_type(4))) float f32x4;
typedef __attribute__((ext_vector_type(2))) float f32x2;
typedef __attribute__((ext_vector_type(4))) unsigned u32x4;
typedef __attribute__((ext_vector_type(2))) unsigned u32x2;

__device__ __forceinline__ float xrow16_max(float x) {
  auto s = __builtin_amdgcn_permlane16_swap(__float_as_uint(x), __float_as_uint(x), false, false);
  x = fmaxf(__uint_as_float(s[0]), __uint_as_float(s[1]));
  auto t = __builtin_amdgcn_permlane32_swap(__float_as_uint(x), __float_as_uint(x), false, false);
  return fmaxf(__uint_as_float(t[0]), __uint_as_float(t[1]));
}
__device__ __forceinline__ float xrow16_sum(float x) {
  auto s = __builtin_amdgcn_permlane16_swap(__float_as_uint(x), __float_as_uint(x), false, false);
  x = __uint_as_float(s[0]) + __uint_as_float(s[1]);
  auto t = __builtin_amdgcn_permlane32_swap(__float_as_uint(x), __float_as_uint(x), false, false);
  return __uint_as_float(t[0]) + __uint_as_float(t[1]);
}
template <int CTRL, int RM = 0xf> __device__ __forceinline__ int dpp_add(int x) {
  return x + __builtin_amdgcn_update_dpp(0, x, CTRL, RM, 0xf, false);
}
__device__ __forceinline__ unsigned umin3(unsigned a, unsigned b, unsigned c) { return min(min(a, b), c); }
__device__ __forceinline__ unsigned lds_or(int* p, unsigned v) { return __hip_atomic_fetch_or((unsigned*)p, v, __ATOMIC_RELAXED, __HIP_MEMORY_SCOPE_WORKGROUP); }
__device__ __forceinline__ int lds_add(int* p, int v) { return __hip_atomic_fetch_add(p, v, __ATOMIC_RELAXED, __HIP_MEMORY_SCOPE_WORKGROUP); }

__device__ __forceinline__ void dma_tile(__amdgpu_buffer_rsrc_t srd, unsigned voff, unsigned soff, unsigned m0v) {
  const unsigned soff1 = soff + 8u * TOKB;
  asm volatile(
      "s_mov_b32 m0, %0\n\ts_nop 0\n\t"
      "buffer_load_dwordx4 %1, %2, %3 offen nt lds\n\t"
      "s_add_u32 m0, m0, 1024\n\ts_nop 0\n\t"
      "buffer_load_dwordx4 %1, %2, %4 offen nt lds\n\t"
      "s_add_u32 m0, m0, 896\n\ts_nop 0\n\t"
      "buffer_load_dwordx4 %1, %2, %3 offen offset:128 nt lds\n\t"
      "s_add_u32 m0, m0, 1024\n\ts_nop 0\n\t"
      "buffer_load_dwordx4 %1, %2, %4 offen offset:128 nt lds\n\t"
      "s_add_u32 m0, m0, 896\n\ts_nop 0\n\t"
      "buffer_load_dwordx4 %1, %2, %3 offen offset:256 nt lds\n\t"
      "s_add_u32 m0, m0, 1024\n\ts_nop 0\n\t"
      "buffer_load_dwordx4 %1, %2, %4 offen offset:256 nt lds"
      :: "s"(m0v), "v"(voff), "s"(srd), "s"(soff), "s"(soff1) : "memory", "scc");
}
template <int N> __device__ __forceinline__ void wait_vm() { asm volatile("s_waitcnt vmcnt(%0)" :: "i"(N) : "memory"); }
__device__ __forceinline__ void wait_lds() { asm volatile("s_waitcnt lgkmcnt(0)" ::: "memory"); }

#define STAMP(i)

__global__ void __launch_bounds__(NTHR, 2) attn_kernel(const __hip_bfloat16* __restrict__ q, const __hip_bfloat16* __restrict__ kc,
                                                       const __hip_bfloat16* __restrict__ vc, const int* __restrict__ bt,
                                                       const int* __restrict__ ctxl, __hip_bfloat16* __restrict__ out,
                                                       unsigned char* __restrict__ ws, int nphys) {
  __shared__ __attribute__((aligned(16))) char lds[LDS_BYTES];
  const int tid = threadIdx.x, lane = tid & 63, wave = __builtin_amdgcn_readfirstlane(tid >> 6);
  const int hg = (blockIdx.x >> 3) & (NHG - 1), s = (blockIdx.x & 7) * (NSPL / 8) + (blockIdx.x >> 6);
  const int hh = wave & 3, pl = wave >> 2;
  const int h = hg * HG + hh;
  const int g = lane >> 4, c16 = lane & 15, cq = lane & 7;
  const int bc = s >> 2, hc = hg * HG + (s & 3);
  STAMP(0);

#define FT(i)
  FT(0);
  const int colid = tid & (NB - 1);
  int key[B];
#pragma unroll
  for (int b = 0; b < B; ++b) key[b] = bt[b * NB + colid];
  u32x4 qf[6];
#pragma unroll
  for (int i = 0; i < 6; ++i) qf[i] = *reinterpret_cast<const u32x4*>(q + ((size_t)cq * H + h) * D + 32 * i + 8 * g);
  const int myctx_raw = ctxl[cq];
  GAS unsigned* epoch = (GAS unsigned*)(ws + WS_EPOCH);
  const unsigned atag = __hip_atomic_load(epoch + hg * 32 + (lane & 31), __ATOMIC_RELAXED, __HIP_MEMORY_SCOPE_AGENT) + 1u;
  FT(1);
  int ce[B];
#pragma unroll
  for (int b = 0; b < B; ++b) { const int c = ctxl[b]; ce[b] = (c <= 0 || c > LK) ? LK : c; }

  int* cnt = reinterpret_cast<int*>(lds + LDS_CNT);
  unsigned long long bal[B];
  if (wave < 4) {
    const int c0 = colid * BS;
#pragma unroll
    for (int b = 0; b < B; ++b) {
      const int p = key[b] + ((key[b] >> 31) & nphys);
      key[b] = (c0 < ce[b]) ? p : -1;
    }
    unsigned blo = 0, bhi = 0;
#pragma unroll
    for (int b = 0; b < B; ++b) {
      unsigned dm = (unsigned)(key[b] + 1);
#pragma unroll
      for (int b2 = 0; b2 < b; ++b2) dm = min(dm, (unsigned)(key[b] ^ key[b2]));
      bal[b] = __ballot(dm != 0u);
      asm("s_nop 1\n\tv_writelane_b32 %0, %2, %4\n\tv_writelane_b32 %1, %3, %4"
          : "+v"(blo), "+v"(bhi) : "s"((unsigned)bal[b]), "s"((unsigned)(bal[b] >> 32)), "i"(b));
    }
    *reinterpret_cast<int4*>(lds + LDS_TAB + colid * 32) = make_int4(key[0], key[1], key[2], key[3]);
    *reinterpret_cast<int4*>(lds + LDS_TAB + colid * 32 + 16) = make_int4(key[4], key[5], key[6], key[7]);
    if (lane < B) *reinterpret_cast<u32x2*>(lds + LDS_BAL + (lane * 4 + wave) * 8) = u32x2{blo, bhi};
    if (tid < 12) cnt[tid] = (tid >= 4 && tid < 8) ? 4 : 0;
  } else {
#pragma unroll
    for (int b = 0; b < B; ++b) bal[b] = 0ull;
  }
  FT(4);
  __syncthreads();
  FT(5);
  STAMP(1);
  const u32x2 gb = *reinterpret_cast<const u32x2*>(lds + LDS_BAL + (lane & 31) * 8);
  const int gown = __popc(gb[0]) + __popc(gb[1]);
  int incl = gown;
  incl = dpp_add<0x111>(incl); incl = dpp_add<0x112>(incl); incl = dpp_add<0x114>(incl); incl = dpp_add<0x118>(incl);
  incl = dpp_add<0x142, 0xa>(incl);
  const int excl = incl - gown;
  const int ntot = __builtin_amdgcn_readlane(incl, 31);
  const int lo = (int)(((unsigned)ntot * (unsigned)s) / NSPL), hi = (int)(((unsigned)ntot * (unsigned)(s + 1)) / NSPL);
  const int nloc = hi - lo;
  auto page_of_rank = [&](int r) -> int {
    const int grp = __popcll(__ballot(incl <= r) & 0xffffffffull) & 31;
    const unsigned b0 = __builtin_amdgcn_readlane(gb[0], grp), b1 = __builtin_amdgcn_readlane(gb[1], grp);
    const int k = r - __builtin_amdgcn_readlane(excl, grp);
    const int below = (int)__builtin_amdgcn_mbcnt_hi(b1, __builtin_amdgcn_mbcnt_lo(b0, 0u));
    const unsigned long long sel = __ballot(below == k) & (((unsigned long long)b1 << 32) | b0);
    const int bit = sel ? __builtin_ctzll(sel) : 0;
    return *reinterpret_cast<const int*>(lds + LDS_TAB + ((grp & 3) * 64 + bit) * 32 + (grp >> 2) * 4);
  };
  FT(6);
  bool vA = pl < nloc, vB = 2 + pl < nloc;
  int phA, phB;
  {
    const int xa = page_of_rank(lo + pl), xb = page_of_rank(lo + 2 + pl);
    phA = __builtin_amdgcn_readfirstlane(xa); phB = __builtin_amdgcn_readfirstlane(xb);
  }
  const unsigned kvbytes = (unsigned)nphys * (unsigned)PAGEB;
  const __amdgpu_buffer_rsrc_t srdK = __builtin_amdgcn_make_buffer_rsrc((void*)kc, (short)0, (int)kvbytes, 0x00020000);
  const __amdgpu_buffer_rsrc_t srdV = __builtin_amdgcn_make_buffer_rsrc((void*)vc, (short)0, (int)kvbytes, 0x00020000);
  const unsigned ldsbase = (unsigned)(size_t)(LAS char*)lds;
  const unsigned ring = ldsbase + (unsigned)wave * RINGB;
  const unsigned voff = (unsigned)(lane >> 3) * TOKB + (unsigned)(((lane & 7) ^ ((lane >> 3) & 6)) * 16);
  const unsigned hoff = (unsigned)h * ROWB;
  FT(7);
  STAMP(2);
  if (vA) {
    dma_tile(srdK, voff, (unsigned)phA * (unsigned)PAGEB + hoff, ring);
    dma_tile(srdV, voff, (unsigned)phA * (unsigned)PAGEB + hoff, ring + TILEB);
  }
  if (vB) dma_tile(srdK, voff, (unsigned)phB * (unsigned)PAGEB + hoff, ring + 2 * TILEB);

  if (wave < 4) {
    int pos[B];
#pragma unroll
    for (int b = 0; b < B; ++b) {
      pos[b] = __builtin_amdgcn_readlane(excl, b * 4 + wave) + (int)__builtin_amdgcn_mbcnt_hi((unsigned)(bal[b] >> 32), __builtin_amdgcn_mbcnt_lo((unsigned)bal[b], 0u));
      const int rel = pos[b] - lo;
      if (((bal[b] >> lane) & 1ull) && (unsigned)rel < (unsigned)nloc) {
        int mk = 0;
#pragma unroll
        for (int b2 = b; b2 < B; ++b2) mk |= (key[b2] == key[b]) ? (1 << (16 + b2)) : 0;
        *reinterpret_cast<int2*>(lds + LDS_LIST + rel * 8) = make_int2(key[b], colid | mk);
      }
    }
    int kc_ = key[0];
#pragma unroll
    for (int b = 1; b < B; ++b) if (b == bc) kc_ = key[b];
    int mp = 0;
#pragma unroll
    for (int b = B - 1; b >= 0; --b) if (key[b] == kc_) mp = pos[b];
    unsigned bit = 0;
    if (kc_ >= 0) {
      const int x = NSPL * mp + NSPL - 1;
      int sp = (int)((float)x * (1.0f / (float)ntot));
      const int r = x - sp * ntot;
      sp += (r >= ntot) ? 1 : 0; sp -= (r < 0) ? 1 : 0;
      bit = 1u << sp;
    }
    bit |= __builtin_amdgcn_update_dpp(0, bit, 0x111, 0xf, 0xf, false);
    bit |= __builtin_amdgcn_update_dpp(0, bit, 0x112, 0xf, 0xf, false);
    bit |= __builtin_amdgcn_update_dpp(0, bit, 0x114, 0xf, 0xf, false);
    bit |= __builtin_amdgcn_update_dpp(0, bit, 0x118, 0xf, 0xf, false);
    const unsigned all = __builtin_amdgcn_readlane(bit, 15) | __builtin_amdgcn_readlane(bit, 31) | __builtin_amdgcn_readlane(bit, 47) | __builtin_amdgcn_readlane(bit, 63);
    if (lane == 0) lds_or(cnt, all);
  }
  const int myctx = (myctx_raw <= 0 || myctx_raw > LK) ? LK : myctx_raw;
  {
    const bool zq = (c16 >= B) || (myctx_raw <= 0);
#pragma unroll
    for (int i = 0; i < 6; ++i) { if (zq) qf[i] = u32x4{0u, 0u, 0u, 0u}; asm volatile("" : "+v"(qf[i])); }
  }
  unsigned atag_p = atag;
  asm volatile("" : "+v"(atag_p));
  wait_lds();
  __builtin_amdgcn_s_barrier();
  unsigned pfdummy = 0;
  int mtA, mtB;
  {
    const int ya = *reinterpret_cast<const int*>(lds + LDS_LIST + pl * 8 + 4), yb = *reinterpret_cast<const int*>(lds + LDS_LIST + (2 + pl) * 8 + 4);
    mtA = __builtin_amdgcn_readfirstlane(ya); mtB = __builtin_amdgcn_readfirstlane(yb);
  }

  const __amdgpu_buffer_rsrc_t srdF = __builtin_amdgcn_make_buffer_rsrc((void*)(ws + WS_FLAG), (short)0, (int)(WS_REC - WS_FLAG), 0x00020000);
  unsigned kaddr[2], vaddr[4];
#pragma unroll
  for (int bb = 0; bb < 2; ++bb) kaddr[bb] = ring + c16 * 128 + (((4 * bb + g) ^ (c16 & 6)) * 16);
  {
    const int t = 4 * g + (c16 >> 2), p = lane & 3;
#pragma unroll
    for (int k = 0; k < 4; ++k) vaddr[k] = ring + t * 128 + ((((2 * k) ^ (t & 6)) + (p >> 1)) * 16) + (p & 1) * 8;
  }
  float m = -INFINITY, l = 0.f;
  f32x4 acc[12];
#pragma unroll
  for (int i = 0; i < 12; ++i) acc[i] = f32x4{0.f, 0.f, 0.f, 0.f};
  const unsigned mytag = __builtin_amdgcn_ds_bpermute((hh * 8 + cq) * 4, (int)atag_p);

  unsigned seen = 0;
  auto early = [&]() -> bool {
    unsigned old = 0;
    if (lane == 0) old = lds_or(cnt + 8 + hh, seen | (1u << pl));
    old = __builtin_amdgcn_readfirstlane(old);
    const unsigned wm = (old | seen) >> 16;
    const bool go = ((old >> (1 - pl)) & 1u) && wm != 0u;
    if (go && lane < B && ((wm >> lane) & 1u)) __builtin_amdgcn_raw_buffer_store_b32(mytag, srdF, ((h * B + lane) * NSPL + s) * 4, 0, 16);
    return go;
  };

  unsigned slot = 0;
  const bool nopages = !vA;
  while (vA) {
    int vidx = 0;
    if (vB && lane == 0) vidx = lds_add(cnt + 4 + hh, 1);
    seen |= (unsigned)mtA & 0xff0000u;
    if (vB) wait_vm<12>(); else wait_vm<6>();
    bf16x8 kf[6];
#pragma unroll
    for (int u = 0; u < 6; ++u) kf[u] = *(const LAS bf16x8*)(size_t)(kaddr[u & 1] + slot + (u >> 1) * 2048);
    asm volatile("s_waitcnt lgkmcnt(0)" : "+v"(kf[0]), "+v"(kf[1]), "+v"(kf[2]), "+v"(kf[3]), "+v"(kf[4]), "+v"(kf[5]), "+v"(vidx) :: "memory");
    if (vB) dma_tile(srdV, voff, (unsigned)phB * (unsigned)PAGEB + hoff, ring + slot);
    const int idxC = __builtin_amdgcn_readfirstlane(vidx);
    const bool vC = vB && idxC < nloc;
    int2 ec = *reinterpret_cast<const int2*>(lds + LDS_LIST + (idxC & 63) * 8);
    slot = (slot + TILEB == RINGB) ? 0u : slot + TILEB;
    f32x4 s0 = {0.f, 0.f, 0.f, 0.f}, s1 = {0.f, 0.f, 0.f, 0.f};
#pragma unroll
    for (int u = 0; u < 6; u += 2) {
      s0 = __builtin_amdgcn_mfma_f32_16x16x32_bf16(kf[u], __builtin_bit_cast(bf16x8, qf[u]), s0, 0, 0, 0);
      s1 = __builtin_amdgcn_mfma_f32_16x16x32_bf16(kf[u + 1], __builtin_bit_cast(bf16x8, qf[u + 1]), s1, 0, 0, 0);
    }
    const int nval = ((mtA >> (16 + c16)) & 1) ? (myctx - (mtA & 0xffff) * BS - 4 * g) : 0;
    float sc[4];
#pragma unroll
    for (int r = 0; r < 4; ++r) sc[r] = (r < nval) ? (s0[r] + s1[r]) * SCALE_LOG2E : -INFINITY;
    float tm = fmaxf(fmaxf(sc[0], sc[1]), fmaxf(sc[2], sc[3]));
    tm = xrow16_max(tm);
    const float mn = fmaxf(m, tm);
    const float msub = (mn == -INFINITY) ? 0.f : mn;
    const float alpha = __builtin_amdgcn_exp2f(m - msub);
    float p[4];
#pragma unroll
    for (int r = 0; r < 4; ++r) p[r] = __builtin_amdgcn_exp2f(sc[r] - msub);
    l = l * alpha + ((p[0] + p[1]) + (p[2] + p[3]));
    if (__any(mn > m)) {
#pragma unroll
      for (int dt = 0; dt < 12; ++dt) acc[dt] *= alpha;
    }
    m = mn;
    const bf16x4 pb4 = {(__bf16)p[0], (__bf16)p[1], (__bf16)p[2], (__bf16)p[3]};
    const s16x4 pf = __builtin_bit_cast(s16x4, pb4);
    if (vB) wait_vm<12>();
    else { wait_vm<0>(); early(); }
    s16x4 vf[12];
#pragma unroll
    for (int dt = 0; dt < 12; ++dt)
      vf[dt] = __builtin_amdgcn_ds_read_tr16_b64_v4i16((LAS s16x4*)(size_t)(vaddr[dt & 3] + slot + (dt >> 2) * 2048));
    asm volatile("s_waitcnt lgkmcnt(0)"
                 : "+v"(vf[0]), "+v"(vf[1]), "+v"(vf[2]), "+v"(vf[3]), "+v"(vf[4]), "+v"(vf[5]), "+v"(vf[6]), "+v"(vf[7]), "+v"(vf[8]), "+v"(vf[9]), "+v"(vf[10]), "+v"(vf[11]),
                   "+v"(ec.x), "+v"(ec.y)
                 :: "memory");
    const int phC = __builtin_amdgcn_readfirstlane(ec.x), mtC = __builtin_amdgcn_readfirstlane(ec.y);
    if (vC) dma_tile(srdK, voff, (unsigned)phC * (unsigned)PAGEB + hoff, ring + slot);
    slot = (slot + TILEB == RINGB) ? 0u : slot + TILEB;
#pragma unroll
    for (int dt = 0; dt < 12; ++dt) acc[dt] = __builtin_amdgcn_mfma_f32_16x16x16bf16_1k(vf[dt], pf, acc[dt], 0, 0, 0);
    phA = phB; mtA = mtB; vA = vB;
    phB = phC; mtB = mtC; vB = vC;
  }
  if (nopages) early();
  asm volatile("" :: "v"(pfdummy));
  STAMP(3);

  const __amdgpu_buffer_rsrc_t srdR = __builtin_amdgcn_make_buffer_rsrc((void*)(ws + WS_REC), (short)0, (int)(WS_END - WS_REC), 0x00020000);
  l = xrow16_sum(l);
  {
    float* xp = reinterpret_cast<float*>(lds + wave * RINGB);
    if (c16 < B) {
      float* dst = xp + c16 * PROW + 4 * g;
#pragma unroll
      for (int dt = 0; dt < 12; ++dt) *reinterpret_cast<f32x4*>(dst + 16 * dt) = acc[dt];
      if (g == 0) *reinterpret_cast<f32x2*>(xp + c16 * PROW + D) = f32x2{m, l};
    }
  }
  wait_lds();
  unsigned pw = 0;
  if (lane == 0) pw = lds_or(cnt + 8 + hh, (4u << pl) | seen);
  pw = __builtin_amdgcn_readfirstlane(pw) | seen;
  asm volatile("" ::: "memory");
  if ((pw >> (3 - pl)) & 1u) {
    const float* xa = reinterpret_cast<const float*>(lds + hh * RINGB);
    const float* xb = reinterpret_cast<const float*>(lds + (hh + 4) * RINGB);
#pragma unroll
    for (int half = 0; half < 2; ++half) {
      if (((pw >> (16 + 4 * half)) & 0xfu) == 0u) continue;
      f32x2 mla[4], mlb[4]; float a[4][3], c[4][3];
#pragma unroll
      for (int k = 0; k < 4; ++k) {
        const int qq = half * 4 + k;
        mla[k] = *reinterpret_cast<const f32x2*>(xa + qq * PROW + D); mlb[k] = *reinterpret_cast<const f32x2*>(xb + qq * PROW + D);
#pragma unroll
        for (int j = 0; j < 3; ++j) { a[k][j] = xa[qq * PROW + lane + 64 * j]; c[k][j] = xb[qq * PROW + lane + 64 * j]; }
      }
#pragma unroll
      for (int k = 0; k < 4; ++k) {
        const int qq = half * 4 + k;
        if ((pw >> (16 + qq)) & 1u) {
          const float M = fmaxf(mla[k][0], mlb[k][0]);
          const float wa = mla[k][1] > 0.f ? __builtin_amdgcn_exp2f(mla[k][0] - M) : 0.f, wb = mlb[k][1] > 0.f ? __builtin_amdgcn_exp2f(mlb[k][0] - M) : 0.f;
          const float L = wa * mla[k][1] + wb * mlb[k][1];
          const float inv = __builtin_amdgcn_rcpf(L);
          const unsigned lse = __float_as_uint(M + __builtin_amdgcn_logf(L));
          const unsigned tag = __builtin_amdgcn_readlane(atag_p, hh * 8 + qq);
          const unsigned tw = (tag << 8) | ((lse >> (8 * (lane & 3))) & 0xffu);
          const float sa = wa * inv, sb = wb * inv;
          const u32x4 gr = {__float_as_uint(sa * a[k][0] + sb * c[k][0]), __float_as_uint(sa * a[k][1] + sb * c[k][1]), __float_as_uint(sa * a[k][2] + sb * c[k][2]), tw};
          __builtin_amdgcn_raw_buffer_store_b128(gr, srdR, ((h * B + qq) * NSPL + s) * RECB + lane * 16, 0, PAUX);
        }
      }
    }
    STAMP(4);
    return;
  }
  STAMP(4);
  {
    int tk = 0;
    if (lane == 0) tk = lds_add(cnt + 1, 1);
    if (__builtin_amdgcn_readfirstlane(tk) != 0) return;
  }
  {
    const unsigned touch = (unsigned)__builtin_amdgcn_readfirstlane(cnt[0]);
    const unsigned ct = __builtin_amdgcn_readlane(atag_p, (s & 3) * 8 + bc);
    const unsigned ct24 = ct & 0xffffffu;
    float M = -INFINITY, L = 0.f, o[3] = {0.f, 0.f, 0.f};
    unsigned pend = touch, armed = 0;
    const unsigned long long t0 = __builtin_amdgcn_s_memrealtime();
    const int recbase = (hc * B + bc) * NSPL;
    while (pend) {
      asm volatile("" ::: "memory");
      unsigned fl = ct;
      if ((pend & ~armed) && lane < 32) fl = __builtin_amdgcn_raw_buffer_load_b32(srdF, (recbase + lane) * 4, 0, 16);
      int sp[NF]; u32x4 a[NF];
      unsigned rest = pend & armed;
#pragma unroll
      for (int r = 0; r < NF; ++r) {
        sp[r] = rest ? __builtin_ctz(rest) : -1; rest &= rest - 1u;
        if (sp[r] >= 0) a[r] = __builtin_amdgcn_raw_buffer_load_b128(srdR, (recbase + sp[r]) * RECB + lane * 16, 0, 16);
        else a[r] = u32x4{0u, 0u, 0u, 0u};
      }
      unsigned done = 0;
#pragma unroll
      for (int r = 0; r < NF; ++r) {
        if (sp[r] >= 0) {
          if (__all((a[r][3] >> 8) == ct24)) {
            done |= 1u << sp[r];
            unsigned x = (a[r][3] & 0xffu) << (8 * (lane & 3));
            x |= __builtin_amdgcn_update_dpp(0u, x, 0xb1, 0xf, 0xf, false);
            x |= __builtin_amdgcn_update_dpp(0u, x, 0x4e, 0xf, 0xf, false);
            const float lse = __uint_as_float(x);
            const float Mn = fmaxf(M, lse);
            const float wo = __builtin_amdgcn_exp2f(M - Mn), wn = __builtin_amdgcn_exp2f(lse - Mn);
#pragma unroll
            for (int j = 0; j < 3; ++j) o[j] = wo * o[j] + wn * __uint_as_float(a[r][j]);
            L = wo * L + wn; M = Mn;
          }
        }
      }
      pend &= ~done;
      const unsigned was = armed;
      armed |= (unsigned)__ballot(lane < 32 && fl == ct);
      if (!done && pend) {
        if (__builtin_amdgcn_s_memrealtime() - t0 > 200000ull) break;
        if (pend & was) __builtin_amdgcn_s_sleep(FULLSLEEP); else if (!(pend & armed)) __builtin_amdgcn_s_sleep(POLLSLEEP);
      }
    }
    STAMP(5);
    const float inv = 1.f / L;
    __hip_bfloat16* orow = out + ((size_t)bc * H + hc) * D;
#pragma unroll
    for (int j = 0; j < 3; ++j) *reinterpret_cast<__bf16*>(orow + lane + 64 * j) = (__bf16)(o[j] * inv);
    if (lane == 0) __hip_atomic_store(epoch + hc * B + bc, ct, __ATOMIC_RELAXED, __HIP_MEMORY_SCOPE_AGENT);
  }
  STAMP(6);
}

extern "C" void kernel_launch(void* const* d_in, const int* in_sizes, int n_in, void* d_out, int out_size, void* d_ws, size_t ws_size,
                              hipStream_t stream) {
  const __hip_bfloat16* q = (const __hip_bfloat16*)d_in[0];
  const __hip_bfloat16* k = (const __hip_bfloat16*)d_in[1];
  const __hip_bfloat16* v = (const __hip_bfloat16*)d_in[2];
  const int* bt = (const int*)d_in[3];
  const int* ctx = (const int*)d_in[4];
  const int nphys = in_sizes[1] / (BS * H * D);
  attn_kernel<<<NWG, NTHR, 0, stream>>>(q, k, v, bt, ctx, (__hip_bfloat16*)d_out, (unsigned char*)d_ws, nphys);
}
```

```cpp
#include <hip/hip_runtime.h>
#include <hip/hip_bf16.h>
#include <cstdio>
#include <cstdint>

#define POLLSLEEP 8
#define FULLSLEEP 1
#define KMAP 0
#define PAUX 16
#define FLAGAT 1
#define TLBPF 0
#define BARLOAD 1
#define LATE03 0
#define NF 8

constexpr int B = 8, H = 32, D = 192, NB = 256, BS = 16, LK = NB * BS;
constexpr int ROWB = D * 2;
constexpr int TOKB = H * ROWB;
constexpr int PAGEB = BS * TOKB;
constexpr int W = 8, HG = 4, NHG = H / HG, NSPL = 32, NWG = NHG * NSPL, NTHR = W * 64;
constexpr int NS = 3;
constexpr int TILEB = BS * ROWB;
constexpr int RINGB = NS * TILEB;
constexpr int PROW = 200;
constexpr int NPRE = 4;
constexpr int LDS_PRE = W * RINGB;
constexpr int LDS_LIST = LDS_PRE + NPRE * PROW * 4;
constexpr int LDS_BAL = LDS_LIST + 64 * 8;
constexpr int LDS_CNT = LDS_BAL + 32 * 8;
constexpr int LDS_TAG = LDS_CNT + 16 * 4;
constexpr int LDS_BYTES = LDS_TAG + W * 256;
static_assert(LDS_BYTES <= 160 * 1024, "LDS");
static_assert(B * PROW * 4 <= RINGB, "epilogue area");
constexpr int RECB = 1024;
constexpr size_t WS_EPOCH = 0, WS_FLAG = 4096, WS_REC = WS_FLAG + (size_t)H * B * NSPL * 4, WS_END = WS_REC + (size_t)H * B * NSPL * RECB;
constexpr float SCALE_LOG2E = 0.07216878364870322f * 1.4426950408889634f;

#define LAS __attribute__((address_space(3)))
#define GAS __attribute__((address_space(1)))
typedef __attribute__((ext_vector_type(8))) __bf16 bf16x8;
typedef __attribute__((ext_vector_type(4))) __bf16 bf16x4;
typedef __attribute__((ext_vector_type(4))) short s16x4;
typedef __attribute__((ext_vector_type(4))) float f32x4;
typedef __attribute__((ext_vector_type(2))) float f32x2;
typedef __attribute__((ext_vector_type(4))) unsigned u32x4;
typedef __attribute__((ext_vector_type(2))) unsigned u32x2;
typedef __attribute__((ext_vector_type(8))) int i32x8;

__device__ __forceinline__ float xrow16_max(float x) {
  auto s = __builtin_amdgcn_permlane16_swap(__float_as_uint(x), __float_as_uint(x), false, false);
  x = fmaxf(__uint_as_float(s[0]), __uint_as_float(s[1]));
  auto t = __builtin_amdgcn_permlane32_swap(__float_as_uint(x), __float_as_uint(x), false, false);
  return fmaxf(__uint_as_float(t[0]), __uint_as_float(t[1]));
}
__device__ __forceinline__ float xrow16_sum(float x) {
  auto s = __builtin_amdgcn_permlane16_swap(__float_as_uint(x), __float_as_uint(x), false, false);
  x = __uint_as_float(s[0]) + __uint_as_float(s[1]);
  auto t = __builtin_amdgcn_permlane32_swap(__float_as_uint(x), __float_as_uint(x), false, false);
  return __uint_as_float(t[0]) + __uint_as_float(t[1]);
}
template <int CTRL, int RM = 0xf> __device__ __forceinline__ int dpp_add(int x) {
  return x + __builtin_amdgcn_update_dpp(0, x, CTRL, RM, 0xf, false);
}
__device__ __forceinline__ unsigned umin3(unsigned a, unsigned b, unsigned c) { return min(min(a, b), c); }
__device__ __forceinline__ unsigned lds_or(int* p, unsigned v) { return __hip_atomic_fetch_or((unsigned*)p, v, __ATOMIC_RELAXED, __HIP_MEMORY_SCOPE_WORKGROUP); }
__device__ __forceinline__ int lds_add(int* p, int v) { return __hip_atomic_fetch_add(p, v, __ATOMIC_RELAXED, __HIP_MEMORY_SCOPE_WORKGROUP); }

__device__ __forceinline__ void dma_tile(__amdgpu_buffer_rsrc_t srd, unsigned voff, unsigned soff, unsigned m0v) {
  const unsigned soff1 = soff + 8u * TOKB;
  asm volatile(
      "s_mov_b32 m0, %0\n\ts_nop 0\n\t"
      "buffer_load_dwordx4 %1, %2, %3 offen nt lds\n\t"
      "s_add_u32 m0, m0, 1024\n\ts_nop 0\n\t"
      "buffer_load_dwordx4 %1, %2, %4 offen nt lds\n\t"
      "s_add_u32 m0, m0, 896\n\ts_nop 0\n\t"
      "buffer_load_dwordx4 %1, %2, %3 offen offset:128 nt lds\n\t"
      "s_add_u32 m0, m0, 1024\n\ts_nop 0\n\t"
      "buffer_load_dwordx4 %1, %2, %4 offen offset:128 nt lds\n\t"
      "s_add_u32 m0, m0, 896\n\ts_nop 0\n\t"
      "buffer_load_dwordx4 %1, %2, %3 offen offset:256 nt lds\n\t"
      "s_add_u32 m0, m0, 1024\n\ts_nop 0\n\t"
      "buffer_load_dwordx4 %1, %2, %4 offen offset:256 nt lds"
      :: "s"(m0v), "v"(voff), "s"(srd), "s"(soff), "s"(soff1) : "memory", "scc");
}
template <int N> __device__ __forceinline__ void wait_vm() { asm volatile("s_waitcnt vmcnt(%0)" :: "i"(N) : "memory"); }
__device__ __forceinline__ void wait_lds() { asm volatile("s_waitcnt lgkmcnt(0)" ::: "memory"); }

#define STAMP(i)
#define STAMPB(i)
#define FLUSH()

__global__ void __launch_bounds__(NTHR, 2) attn_kernel(const __hip_bfloat16* __restrict__ q, const __hip_bfloat16* __restrict__ kc,
                                                       const __hip_bfloat16* __restrict__ vc, const int* __restrict__ bt,
                                                       const int* __restrict__ ctxl, __hip_bfloat16* __restrict__ out,
                                                       unsigned char* __restrict__ ws, int nphys) {
#define FT(i)
#define FTB(i)
  FT(0);
  STAMP(0);
  __shared__ __attribute__((aligned(16))) char lds[LDS_BYTES];
  const int tid = threadIdx.x, lane = tid & 63, wave = __builtin_amdgcn_readfirstlane(tid >> 6);
  const int hg = (blockIdx.x >> 3) & (NHG - 1), s = (blockIdx.x & 7) * (NSPL / 8) + (blockIdx.x >> 6);
  const int hh = wave & 3, pl = wave >> 2;
  const int h = hg * HG + hh;
  const int g = lane >> 4, c16 = lane & 15, cq = lane & 7;
  const int bc = s >> 2, hc = hg * HG + (s & 3);

  const int colid = tid & (NB - 1);
  i32x8 ce; int cbc;
  asm volatile("s_load_dwordx8 %0, %2, 0x0\n\ts_load_dword %1, %2, %3" : "=&s"(ce), "=&s"(cbc) : "s"(ctxl), "s"(bc * 4));
  int key[B];
  u32x4 rowc;
  asm volatile("s_cmp_ge_u32 %13, 4\n\ts_cbranch_scc1 1f\n\t"
               "global_load_dword %1, %9, %10\n\tglobal_load_dword %2, %9, %10 offset:1024\n\tglobal_load_dword %3, %9, %10 offset:2048\n\tglobal_load_dword %4, %9, %10 offset:3072\n\t"
               "global_load_dword %5, %9, %11\n\tglobal_load_dword %6, %9, %11 offset:1024\n\tglobal_load_dword %7, %9, %11 offset:2048\n\tglobal_load_dword %8, %9, %11 offset:3072\n\t"
               "s_branch 2f\n1:\n\tglobal_load_dwordx4 %0, %14, %12\n2:"
               : "=&v"(rowc), "=&v"(key[0]), "=&v"(key[1]), "=&v"(key[2]), "=&v"(key[3]), "=&v"(key[4]), "=&v"(key[5]), "=&v"(key[6]), "=&v"(key[7])
               : "v"(colid * 4), "s"(bt), "s"(bt + 4 * NB), "s"(bt + bc * NB), "s"(wave), "v"(lane * 16) : "scc");
  const unsigned ldsbase = (unsigned)(size_t)(LAS char*)lds;
  const unsigned ring = ldsbase + (unsigned)wave * RINGB;
  {
    const __amdgpu_buffer_rsrc_t srdQ = __builtin_amdgcn_make_buffer_rsrc((void*)q, (short)0, B * H * ROWB, 0x00020000);
    const __amdgpu_buffer_rsrc_t srdE = __builtin_amdgcn_make_buffer_rsrc((void*)(ws + WS_EPOCH), (short)0, H * B * 4, 0x00020000);
    unsigned vo[4];
#pragma unroll
    for (int k = 0; k < 3; ++k) { const unsigned idx = k * 64 + lane, row = (idx * 2731u) >> 16; vo[k] = row * (unsigned)TOKB + (idx - row * 24u) * 16u; }
    vo[3] = (unsigned)(lane - ((lane * 2731) >> 16) * 24) * 16u;
    asm volatile("s_mov_b32 m0, %0\n\ts_nop 0\n\t"
                 "buffer_load_dwordx4 %1, %5, %6 offen lds\n\t"
                 "s_add_u32 m0, m0, 1024\n\ts_nop 0\n\t"
                 "buffer_load_dwordx4 %2, %5, %6 offen lds\n\t"
                 "s_add_u32 m0, m0, 1024\n\ts_nop 0\n\t"
                 "buffer_load_dwordx4 %3, %5, %6 offen lds\n\t"
                 "s_add_u32 m0, m0, 1024\n\ts_nop 0\n\t"
                 "buffer_load_dwordx4 %4, %5, %7 offen lds\n\t"
                 "s_mov_b32 m0, %8\n\ts_nop 0\n\t"
                 "buffer_load_dword %9, %10, %11 offen sc1 lds"
                 :: "s"(ring + 2 * TILEB), "v"(vo[0]), "v"(vo[1]), "v"(vo[2]), "v"(vo[3]), "s"(srdQ), "s"(h * ROWB), "s"((bc * H + hc) * ROWB),
                    "s"(ldsbase + LDS_TAG + wave * 256), "v"((lane & 31) * 4), "s"(srdE), "s"(hg * 128)
                 : "memory", "scc");
  }
  FT(1);
  const unsigned kvbytes = (unsigned)nphys * (unsigned)PAGEB;
  const __amdgpu_buffer_rsrc_t srdK = __builtin_amdgcn_make_buffer_rsrc((void*)kc, (short)0, (int)kvbytes, 0x00020000);
  const __amdgpu_buffer_rsrc_t srdV = __builtin_amdgcn_make_buffer_rsrc((void*)vc, (short)0, (int)kvbytes, 0x00020000);
  const unsigned voff = (unsigned)(lane >> 3) * TOKB + (unsigned)(((lane & 7) ^ ((lane >> 3) & 6)) * 16);
  const unsigned hoff = (unsigned)h * ROWB;
  int* cnt = reinterpret_cast<int*>(lds + LDS_CNT);
  asm volatile("s_waitcnt lgkmcnt(0)" : "+s"(ce), "+s"(cbc) :: "memory");
  const int npb = (((cbc <= 0 || cbc > LK) ? LK : cbc) + BS - 1) / BS;
  const int colp = npb - NPRE + (wave - 4);
  const bool vP = wave >= 4 && colp >= 0;
  int phA = 0, mtA = (colp & 0xffff) | (1 << (16 + bc));
  unsigned long long bal[B];
  asm volatile("s_waitcnt vmcnt(5)" : "+v"(rowc), "+v"(key[0]), "+v"(key[1]), "+v"(key[2]), "+v"(key[3]), "+v"(key[4]), "+v"(key[5]), "+v"(key[6]), "+v"(key[7]) :: "memory");
  if (wave >= 4) {
    const int x01 = (int)((colp & 1) ? rowc[1] : rowc[0]), x23 = (int)((colp & 1) ? rowc[3] : rowc[2]);
    const int p = __builtin_amdgcn_readlane((colp & 2) ? x23 : x01, (colp >> 2) & 63);
    phA = p + ((p >> 31) & nphys);
    FTB(2);
    STAMPB(2);
    if (vP) {
      dma_tile(srdK, voff, (unsigned)phA * (unsigned)PAGEB + (unsigned)hc * ROWB, ring);
      dma_tile(srdV, voff, (unsigned)phA * (unsigned)PAGEB + (unsigned)hc * ROWB, ring + TILEB);
    }
    FTB(3);
#pragma unroll
    for (int b = 0; b < B; ++b) { bal[b] = 0ull; key[b] = -1; }
  } else {
    const int c0 = (colid + NPRE) * BS;
#pragma unroll
    for (int b = 0; b < B; ++b) {
      const int c = (ce[b] <= 0 || ce[b] > LK) ? LK : ce[b];
      const int p = key[b] + ((key[b] >> 31) & nphys);
      key[b] = (c0 < c) ? p : -1;
    }
    FTB(2);
    unsigned blo = 0, bhi = 0;
#pragma unroll
    for (int b = 0; b < B; ++b) {
      unsigned dm = (unsigned)(key[b] + 1);
#pragma unroll
      for (int b2 = 0; b2 < b; ++b2) dm = min(dm, (unsigned)(key[b] ^ key[b2]));
      bal[b] = __ballot(dm != 0u);
      asm("s_nop 1\n\tv_writelane_b32 %0, %2, %4\n\tv_writelane_b32 %1, %3, %4"
          : "+v"(blo), "+v"(bhi) : "s"((unsigned)bal[b]), "s"((unsigned)(bal[b] >> 32)), "i"(b));
    }
    if (lane < B) *reinterpret_cast<u32x2*>(lds + LDS_BAL + (lane * 4 + wave) * 8) = u32x2{blo, bhi};
    if (tid < 12) cnt[tid] = (tid >= 4 && tid < 8) ? 3 : 0;
    FTB(3);
    STAMPB(2);
  }
  FT(4);
  wait_lds();
  __builtin_amdgcn_s_barrier();
  FT(5);
  STAMP(1);
  const u32x2 gb = *reinterpret_cast<const u32x2*>(lds + LDS_BAL + (lane & 31) * 8);
  const int gown = __popc(gb[0]) + __popc(gb[1]);
  int incl = gown;
  incl = dpp_add<0x111>(incl); incl = dpp_add<0x112>(incl); incl = dpp_add<0x114>(incl); incl = dpp_add<0x118>(incl);
  incl = dpp_add<0x142, 0xa>(incl);
  const int excl = incl - gown;
  const int ntot = __builtin_amdgcn_readlane(incl, 31);
  const int lo = (int)(((unsigned)ntot * (unsigned)s) / NSPL), hi = (int)(((unsigned)ntot * (unsigned)(s + 1)) / NSPL);
  const int nloc = hi - lo;
  FT(6);
  if (wave < 4) {
    int pos[B];
#pragma unroll
    for (int b = 0; b < B; ++b) {
      pos[b] = __builtin_amdgcn_readlane(excl, b * 4 + wave) + (int)__builtin_amdgcn_mbcnt_hi((unsigned)(bal[b] >> 32), __builtin_amdgcn_mbcnt_lo((unsigned)bal[b], 0u));
      const int rel = pos[b] - lo;
      if (((bal[b] >> lane) & 1ull) && (unsigned)rel < (unsigned)nloc) {
        int mk = 0;
#pragma unroll
        for (int b2 = b; b2 < B; ++b2) mk |= (key[b2] == key[b]) ? (1 << (16 + b2)) : 0;
        *reinterpret_cast<int2*>(lds + LDS_LIST + rel * 8) = make_int2(key[b], colid | mk);
      }
    }
    int kc_ = key[0];
#pragma unroll
    for (int b = 1; b < B; ++b) if (b == bc) kc_ = key[b];
    int mp = 0;
#pragma unroll
    for (int b = B - 1; b >= 0; --b) if (key[b] == kc_) mp = pos[b];
    unsigned bit = 0;
    if (kc_ >= 0) {
      const int x = NSPL * mp + NSPL - 1;
      int sp = (int)((float)x * (1.0f / (float)ntot));
      const int r = x - sp * ntot;
      sp += (r >= ntot) ? 1 : 0; sp -= (r < 0) ? 1 : 0;
      bit = 1u << sp;
    }
    bit |= __builtin_amdgcn_update_dpp(0, bit, 0x111, 0xf, 0xf, false);
    bit |= __builtin_amdgcn_update_dpp(0, bit, 0x112, 0xf, 0xf, false);
    bit |= __builtin_amdgcn_update_dpp(0, bit, 0x114, 0xf, 0xf, false);
    bit |= __builtin_amdgcn_update_dpp(0, bit, 0x118, 0xf, 0xf, false);
    const unsigned all = __builtin_amdgcn_readlane(bit, 15) | __builtin_amdgcn_readlane(bit, 31) | __builtin_amdgcn_readlane(bit, 47) | __builtin_amdgcn_readlane(bit, 63);
    if (lane == 0) lds_or(cnt, all);
  }
  wait_lds();
  __builtin_amdgcn_s_barrier();
  bool vA, vB;
  int phB, mtB;
  bool first = vP;
  u32x4 qf[6], qm[6];
  {
    const int2 e0 = *reinterpret_cast<const int2*>(lds + LDS_LIST + pl * 8), e2 = *reinterpret_cast<const int2*>(lds + LDS_LIST + 2 * 8);
    const int p0 = __builtin_amdgcn_readfirstlane(e0.x), m0 = __builtin_amdgcn_readfirstlane(e0.y);
    if (vP) {
      vA = true; vB = 1 < nloc; phB = p0; mtB = m0;
    } else {
      vA = pl < nloc; phA = p0; mtA = m0;
      if (vA) {
        dma_tile(srdK, voff, (unsigned)phA * (unsigned)PAGEB + hoff, ring);
        dma_tile(srdV, voff, (unsigned)phA * (unsigned)PAGEB + hoff, ring + TILEB);
      }
      if (pl == 0) {
        vB = 2 < nloc; phB = __builtin_amdgcn_readfirstlane(e2.x); mtB = __builtin_amdgcn_readfirstlane(e2.y);
      } else {
        int idx = 64;
        if (vA && lane == 0) idx = lds_add(cnt + 4 + hh, 1);
        idx = __builtin_amdgcn_readfirstlane(idx);
        vB = vA && idx < nloc;
        const int2 e1 = *reinterpret_cast<const int2*>(lds + LDS_LIST + (idx & 63) * 8);
        phB = __builtin_amdgcn_readfirstlane(e1.x); mtB = __builtin_amdgcn_readfirstlane(e1.y);
      }
    }
    if (vA) wait_vm<13>(); else wait_vm<0>();
    const char* qs = lds + wave * RINGB + 2 * TILEB;
#pragma unroll
    for (int i = 0; i < 6; ++i) {
      qm[i] = *reinterpret_cast<const u32x4*>(qs + cq * ROWB + i * 64 + g * 16);
      qf[i] = *reinterpret_cast<const u32x4*>(qs + (vP ? B * ROWB : cq * ROWB) + i * 64 + g * 16);
    }
    asm volatile("s_waitcnt lgkmcnt(0)" : "+v"(qf[0]), "+v"(qf[1]), "+v"(qf[2]), "+v"(qf[3]), "+v"(qf[4]), "+v"(qf[5]),
                 "+v"(qm[0]), "+v"(qm[1]), "+v"(qm[2]), "+v"(qm[3]), "+v"(qm[4]), "+v"(qm[5]) :: "memory");
    if (vB) dma_tile(srdK, voff, (unsigned)phB * (unsigned)PAGEB + hoff, ring + 2 * TILEB);
  }
  FT(7);
  int myctx_raw = ce[0];
#pragma unroll
  for (int b = 1; b < B; ++b) myctx_raw = (cq == b) ? ce[b] : myctx_raw;
  const int myctx = (myctx_raw <= 0 || myctx_raw > LK) ? LK : myctx_raw;
  {
    const bool zq = (c16 >= B) || (myctx_raw <= 0);
    const bool zf = vP ? (cbc <= 0) : zq;
#pragma unroll
    for (int i = 0; i < 6; ++i) { if (zq) qm[i] = u32x4{0u, 0u, 0u, 0u}; if (zf) qf[i] = u32x4{0u, 0u, 0u, 0u}; }
  }
  const unsigned* etag = reinterpret_cast<const unsigned*>(lds + LDS_TAG + wave * 256);

  const __amdgpu_buffer_rsrc_t srdF = __builtin_amdgcn_make_buffer_rsrc((void*)(ws + WS_FLAG), (short)0, (int)(WS_REC - WS_FLAG), 0x00020000);
  unsigned kaddr[2], vaddr[4];
#pragma unroll
  for (int bb = 0; bb < 2; ++bb) kaddr[bb] = ring + c16 * 128 + (((4 * bb + g) ^ (c16 & 6)) * 16);
  {
    const int t = 4 * g + (c16 >> 2), p = lane & 3;
#pragma unroll
    for (int k = 0; k < 4; ++k) vaddr[k] = ring + t * 128 + ((((2 * k) ^ (t & 6)) + (p >> 1)) * 16) + (p & 1) * 8;
  }
  float m = -INFINITY, l = 0.f;
  f32x4 acc[12];
#pragma unroll
  for (int i = 0; i < 12; ++i) acc[i] = f32x4{0.f, 0.f, 0.f, 0.f};

  unsigned seen = 0;
  auto early = [&]() -> bool {
    unsigned old = 0;
    if (lane == 0) old = lds_or(cnt + 8 + hh, seen | (1u << pl));
    old = __builtin_amdgcn_readfirstlane(old);
    const unsigned wm = (old | seen) >> 16;
    const bool go = ((old >> (1 - pl)) & 1u) && wm != 0u;
    if (go) {
      const unsigned mytag = etag[hh * 8 + cq] + 1u;
      if (lane < B && ((wm >> lane) & 1u)) __builtin_amdgcn_raw_buffer_store_b32(mytag, srdF, ((h * B + lane) * NSPL + s) * 4, 0, 16);
    }
    return go;
  };

  unsigned slot = 0;
  const bool nopages = !vA;
  while (vA) {
    int vidx = 0;
    if (vB && lane == 0) vidx = lds_add(cnt + 4 + hh, 1);
    seen |= first ? 0u : ((unsigned)mtA & 0xff0000u);
    if (vB) wait_vm<12>(); else wait_vm<6>();
    bf16x8 kf[6];
#pragma unroll
    for (int u = 0; u < 6; ++u) kf[u] = *(const LAS bf16x8*)(size_t)(kaddr[u & 1] + slot + (u >> 1) * 2048);
    asm volatile("s_waitcnt lgkmcnt(0)" : "+v"(kf[0]), "+v"(kf[1]), "+v"(kf[2]), "+v"(kf[3]), "+v"(kf[4]), "+v"(kf[5]), "+v"(vidx) :: "memory");
    if (vB) dma_tile(srdV, voff, (unsigned)phB * (unsigned)PAGEB + hoff, ring + slot);
    const int idxC = __builtin_amdgcn_readfirstlane(vidx);
    const bool vC = vB && idxC < nloc;
    int2 ec = *reinterpret_cast<const int2*>(lds + LDS_LIST + (idxC & 63) * 8);
    slot = (slot + TILEB == RINGB) ? 0u : slot + TILEB;
    f32x4 s0 = {0.f, 0.f, 0.f, 0.f}, s1 = {0.f, 0.f, 0.f, 0.f};
#pragma unroll
    for (int u = 0; u < 6; u += 2) {
      s0 = __builtin_amdgcn_mfma_f32_16x16x32_bf16(kf[u], __builtin_bit_cast(bf16x8, qf[u]), s0, 0, 0, 0);
      s1 = __builtin_amdgcn_mfma_f32_16x16x32_bf16(kf[u + 1], __builtin_bit_cast(bf16x8, qf[u + 1]), s1, 0, 0, 0);
    }
    const int nval = ((mtA >> (16 + c16)) & 1) ? (myctx - (mtA & 0xffff) * BS - 4 * g) : 0;
    float sc[4];
#pragma unroll
    for (int r = 0; r < 4; ++r) sc[r] = (r < nval) ? (s0[r] + s1[r]) * SCALE_LOG2E : -INFINITY;
    float tm = fmaxf(fmaxf(sc[0], sc[1]), fmaxf(sc[2], sc[3]));
    tm = xrow16_max(tm);
    const float mn = fmaxf(m, tm);
    const float msub = (mn == -INFINITY) ? 0.f : mn;
    const float alpha = __builtin_amdgcn_exp2f(m - msub);
    float p[4];
#pragma unroll
    for (int r = 0; r < 4; ++r) p[r] = __builtin_amdgcn_exp2f(sc[r] - msub);
    l = l * alpha + ((p[0] + p[1]) + (p[2] + p[3]));
    if (__any(mn > m)) {
#pragma unroll
      for (int dt = 0; dt < 12; ++dt) acc[dt] *= alpha;
    }
    m = mn;
    const bf16x4 pb4 = {(__bf16)p[0], (__bf16)p[1], (__bf16)p[2], (__bf16)p[3]};
    const s16x4 pf = __builtin_bit_cast(s16x4, pb4);
    if (vB) wait_vm<12>();
    else { wait_vm<0>(); early(); }
    s16x4 vf[12];
#pragma unroll
    for (int dt = 0; dt < 12; ++dt)
      vf[dt] = __builtin_amdgcn_ds_read_tr16_b64_v4i16((LAS s16x4*)(size_t)(vaddr[dt & 3] + slot + (dt >> 2) * 2048));
    asm volatile("s_waitcnt lgkmcnt(0)"
                 : "+v"(vf[0]), "+v"(vf[1]), "+v"(vf[2]), "+v"(vf[3]), "+v"(vf[4]), "+v"(vf[5]), "+v"(vf[6]), "+v"(vf[7]), "+v"(vf[8]), "+v"(vf[9]), "+v"(vf[10]), "+v"(vf[11]),
                   "+v"(ec.x), "+v"(ec.y)
                 :: "memory");
    const int phC = __builtin_amdgcn_readfirstlane(ec.x), mtC = __builtin_amdgcn_readfirstlane(ec.y);
    if (vC) dma_tile(srdK, voff, (unsigned)phC * (unsigned)PAGEB + hoff, ring + slot);
    slot = (slot + TILEB == RINGB) ? 0u : slot + TILEB;
#pragma unroll
    for (int dt = 0; dt < 12; ++dt) acc[dt] = __builtin_amdgcn_mfma_f32_16x16x16bf16_1k(vf[dt], pf, acc[dt], 0, 0, 0);
    if (first) {
      first = false;
      const float ls = xrow16_sum(l);
      float* xp = reinterpret_cast<float*>(lds + LDS_PRE) + (wave - 4) * PROW;
      if (c16 == bc) {
#pragma unroll
        for (int dt = 0; dt < 12; ++dt) *reinterpret_cast<f32x4*>(xp + 16 * dt + 4 * g) = acc[dt];
        if (g == 0) *reinterpret_cast<f32x2*>(xp + D) = f32x2{m, ls};
      }
      wait_lds();
      if (lane == 0) lds_add(cnt + 2, 1);
      m = -INFINITY; l = 0.f;
#pragma unroll
      for (int dt = 0; dt < 12; ++dt) acc[dt] = f32x4{0.f, 0.f, 0.f, 0.f};
#pragma unroll
      for (int i = 0; i < 6; ++i) qf[i] = qm[i];
    }
    phA = phB; mtA = mtB; vA = vB;
    phB = phC; mtB = mtC; vB = vC;
  }
  if (nopages) early();
  STAMP(3);

  const __amdgpu_buffer_rsrc_t srdR = __builtin_amdgcn_make_buffer_rsrc((void*)(ws + WS_REC), (short)0, (int)(WS_END - WS_REC), 0x00020000);
  l = xrow16_sum(l);
  {
    float* xp = reinterpret_cast<float*>(lds + wave * RINGB);
    if (c16 < B) {
      float* dst = xp + c16 * PROW + 4 * g;
#pragma unroll
      for (int dt = 0; dt < 12; ++dt) *reinterpret_cast<f32x4*>(dst + 16 * dt) = acc[dt];
      if (g == 0) *reinterpret_cast<f32x2*>(xp + c16 * PROW + D) = f32x2{m, l};
    }
  }
  wait_lds();
  unsigned pw = 0;
  if (lane == 0) pw = lds_or(cnt + 8 + hh, (4u << pl) | seen);
  pw = __builtin_amdgcn_readfirstlane(pw) | seen;
  asm volatile("" ::: "memory");
  const unsigned eraw = etag[lane & 31];
  if ((pw >> (3 - pl)) & 1u) {
    const float* xa = reinterpret_cast<const float*>(lds + hh * RINGB);
    const float* xb = reinterpret_cast<const float*>(lds + (hh + 4) * RINGB);
#pragma unroll
    for (int half = 0; half < 2; ++half) {
      if (((pw >> (16 + 4 * half)) & 0xfu) == 0u) continue;
      f32x2 mla[4], mlb[4]; float a[4][3], c[4][3];
#pragma unroll
      for (int k = 0; k < 4; ++k) {
        const int qq = half * 4 + k;
        mla[k] = *reinterpret_cast<const f32x2*>(xa + qq * PROW + D); mlb[k] = *reinterpret_cast<const f32x2*>(xb + qq * PROW + D);
#pragma unroll
        for (int j = 0; j < 3; ++j) { a[k][j] = xa[qq * PROW + lane + 64 * j]; c[k][j] = xb[qq * PROW + lane + 64 * j]; }
      }
#pragma unroll
      for (int k = 0; k < 4; ++k) {
        const int qq = half * 4 + k;
        if ((pw >> (16 + qq)) & 1u) {
          const float M = fmaxf(mla[k][0], mlb[k][0]);
          const float wa = mla[k][1] > 0.f ? __builtin_amdgcn_exp2f(mla[k][0] - M) : 0.f, wb = mlb[k][1] > 0.f ? __builtin_amdgcn_exp2f(mlb[k][0] - M) : 0.f;
          const float L = wa * mla[k][1] + wb * mlb[k][1];
          const float inv = __builtin_amdgcn_rcpf(L);
          const unsigned lse = __float_as_uint(M + __builtin_amdgcn_logf(L));
          const unsigned tag = __builtin_amdgcn_readlane(eraw, hh * 8 + qq) + 1u;
          const unsigned tw = (tag << 8) | ((lse >> (8 * (lane & 3))) & 0xffu);
          const float sa = wa * inv, sb = wb * inv;
          const u32x4 gr = {__float_as_uint(sa * a[k][0] + sb * c[k][0]), __float_as_uint(sa * a[k][1] + sb * c[k][1]), __float_as_uint(sa * a[k][2] + sb * c[k][2]), tw};
          __builtin_amdgcn_raw_buffer_store_b128(gr, srdR, ((h * B + qq) * NSPL + s) * RECB + lane * 16, 0, PAUX);
        }
      }
    }
    STAMP(4);
    FLUSH();
    return;
  }
  STAMP(4);
  {
    int tk = 0;
    if (lane == 0) tk = lds_add(cnt + 1, 1);
    if (__builtin_amdgcn_readfirstlane(tk) != 0) { FLUSH(); return; }
  }
  {
    const unsigned touch = (unsigned)__builtin_amdgcn_readfirstlane(cnt[0]);
    const unsigned ct = __builtin_amdgcn_readlane(eraw, (s & 3) * 8 + bc) + 1u;
    const unsigned ct24 = ct & 0xffffffu;
    float M = -INFINITY, L = 0.f, o[3] = {0.f, 0.f, 0.f};
    unsigned pend = touch, armed = 0;
    const unsigned long long t0 = __builtin_amdgcn_s_memrealtime();
    {
      const int nv = min(npb, NPRE);
      while (__hip_atomic_load(cnt + 2, __ATOMIC_RELAXED, __HIP_MEMORY_SCOPE_WORKGROUP) < nv) __builtin_amdgcn_s_sleep(1);
      asm volatile("" ::: "memory");
      const float* xp = reinterpret_cast<const float*>(lds + LDS_PRE);
      f32x2 ml[NPRE]; float x[NPRE][3];
#pragma unroll
      for (int w = 0; w < NPRE; ++w) {
        ml[w] = *reinterpret_cast<const f32x2*>(xp + w * PROW + D);
#pragma unroll
        for (int j = 0; j < 3; ++j) x[w][j] = xp[w * PROW + lane + 64 * j];
      }
#pragma unroll
      for (int w = 0; w < NPRE; ++w) if (w >= NPRE - nv) M = fmaxf(M, ml[w][0]);
#pragma unroll
      for (int w = 0; w < NPRE; ++w) {
        if (w >= NPRE - nv) {
          const float wt = __builtin_amdgcn_exp2f(ml[w][0] - M);
          L += wt * ml[w][1];
#pragma unroll
          for (int j = 0; j < 3; ++j) o[j] += wt * x[w][j];
        }
      }
    }
    const int recbase = (hc * B + bc) * NSPL;
    while (pend) {
      asm volatile("" ::: "memory");
      unsigned fl = ct;
      if ((pend & ~armed) && lane < 32) fl = __builtin_amdgcn_raw_buffer_load_b32(srdF, (recbase + lane) * 4, 0, 16);
      int sp[NF]; u32x4 a[NF];
      unsigned rest = pend & armed;
#pragma unroll
      for (int r = 0; r < NF; ++r) {
        sp[r] = rest ? __builtin_ctz(rest) : -1; rest &= rest - 1u;
        if (sp[r] >= 0) a[r] = __builtin_amdgcn_raw_buffer_load_b128(srdR, (recbase + sp[r]) * RECB + lane * 16, 0, 16);
        else a[r] = u32x4{0u, 0u, 0u, 0u};
      }
      unsigned done = 0;
#pragma unroll
      for (int r = 0; r < NF; ++r) {
        if (sp[r] >= 0) {
          if (__all((a[r][3] >> 8) == ct24)) {
            done |= 1u << sp[r];
            unsigned x = (a[r][3] & 0xffu) << (8 * (lane & 3));
            x |= __builtin_amdgcn_update_dpp(0u, x, 0xb1, 0xf, 0xf, false);
            x |= __builtin_amdgcn_update_dpp(0u, x, 0x4e, 0xf, 0xf, false);
            const float lse = __uint_as_float(x);
            const float Mn = fmaxf(M, lse);
            const float wo = __builtin_amdgcn_exp2f(M - Mn), wn = __builtin_amdgcn_exp2f(lse - Mn);
#pragma unroll
            for (int j = 0; j < 3; ++j) o[j] = wo * o[j] + wn * __uint_as_float(a[r][j]);
            L = wo * L + wn; M = Mn;
          }
        }
      }
      pend &= ~done;
      const unsigned was = armed;
      armed |= (unsigned)__ballot(lane < 32 && fl == ct);
      if (!done && pend) {
        if (__builtin_amdgcn_s_memrealtime() - t0 > 200000ull) break;
        if (pend & was) __builtin_amdgcn_s_sleep(FULLSLEEP); else if (!(pend & armed)) __builtin_amdgcn_s_sleep(POLLSLEEP);
      }
    }
    STAMP(5);
    const float inv = 1.f / L;
    __hip_bfloat16* orow = out + ((size_t)bc * H + hc) * D;
#pragma unroll
    for (int j = 0; j < 3; ++j) *reinterpret_cast<__bf16*>(orow + lane + 64 * j) = (__bf16)(o[j] * inv);
    if (lane == 0) __hip_atomic_store((GAS unsigned*)(ws + WS_EPOCH) + hc * B + bc, ct, __ATOMIC_RELAXED, __HIP_MEMORY_SCOPE_AGENT);
  }
  STAMP(6);
  FLUSH();
}

extern "C" void kernel_launch(void* const* d_in, const int* in_sizes, int n_in, void* d_out, int out_size, void* d_ws, size_t ws_size,
                              hipStream_t stream) {
  const __hip_bfloat16* q = (const __hip_bfloat16*)d_in[0];
  const __hip_bfloat16* k = (const __hip_bfloat16*)d_in[1];
  const __hip_bfloat16* v = (const __hip_bfloat16*)d_in[2];
  const int* bt = (const int*)d_in[3];
  const int* ctx = (const int*)d_in[4];
  const int nphys = in_sizes[1] / (BS * H * D);
  attn_kernel<<<NWG, NTHR, 0, stream>>>(q, k, v, bt, ctx, (__hip_bfloat16*)d_out, (unsigned char*)d_ws, nphys);
}
```

```cpp
#include <hip/hip_runtime.h>
#include <hip/hip_bf16.h>
#include <cstdint>

constexpr int B = 8, H = 32, D = 192, NB = 256, BS = 16, LK = NB * BS;
constexpr int ROWB = D * 2;
constexpr int TOKB = H * ROWB;
constexpr int PAGEB = BS * TOKB;
constexpr int W = 8, HG = 4, NHG = H / HG, NSPL = 32, NWG = NHG * NSPL, NTHR = W * 64;
constexpr int NS = 3;
constexpr int TILEB = BS * ROWB;
constexpr int RINGB = NS * TILEB;
constexpr int PROW = 200;
constexpr int NPRE = 4;
constexpr int LDS_PRE = W * RINGB;
constexpr int LDS_LIST = LDS_PRE + NPRE * PROW * 4;
constexpr int LDS_BAL = LDS_LIST + 64 * 8;
constexpr int LDS_CNT = LDS_BAL + 32 * 8;
constexpr int LDS_TAG = LDS_CNT + 16 * 4;
constexpr int LDS_BYTES = LDS_TAG + W * 256;
static_assert(LDS_BYTES <= 160 * 1024, "LDS");
static_assert(B * PROW * 4 <= RINGB, "epilogue area");
constexpr int RECB = 1024;
constexpr size_t WS_EPOCH = 0, WS_FLAG = 4096, WS_REC = WS_FLAG + (size_t)H * B * NSPL * 4, WS_END = WS_REC + (size_t)H * B * NSPL * RECB;
constexpr float SCALE_LOG2E = 0.07216878364870322f * 1.4426950408889634f;

#define LAS __attribute__((address_space(3)))
#define GAS __attribute__((address_space(1)))
typedef __attribute__((ext_vector_type(8))) __bf16 bf16x8;
typedef __attribute__((ext_vector_type(4))) __bf16 bf16x4;
typedef __attribute__((ext_vector_type(4))) short s16x4;
typedef __attribute__((ext_vector_type(4))) float f32x4;
typedef __attribute__((ext_vector_type(2))) float f32x2;
typedef __attribute__((ext_vector_type(4))) unsigned u32x4;
typedef __attribute__((ext_vector_type(2))) unsigned u32x2;
typedef __attribute__((ext_vector_type(8))) int i32x8;

__device__ __forceinline__ float xrow16_max(float x) {
  auto s = __builtin_amdgcn_permlane16_swap(__float_as_uint(x), __float_as_uint(x), false, false);
  x = fmaxf(__uint_as_float(s[0]), __uint_as_float(s[1]));
  auto t = __builtin_amdgcn_permlane32_swap(__float_as_uint(x), __float_as_uint(x), false, false);
  return fmaxf(__uint_as_float(t[0]), __uint_as_float(t[1]));
}
__device__ __forceinline__ float xrow16_sum(float x) {
  auto s = __builtin_amdgcn_permlane16_swap(__float_as_uint(x), __float_as_uint(x), false, false);
  x = __uint_as_float(s[0]) + __uint_as_float(s[1]);
  auto t = __builtin_amdgcn_permlane32_swap(__float_as_uint(x), __float_as_uint(x), false, false);
  return __uint_as_float(t[0]) + __uint_as_float(t[1]);
}
template <int CTRL, int RM = 0xf> __device__ __forceinline__ int dpp_add(int x) {
  return x + __builtin_amdgcn_update_dpp(0, x, CTRL, RM, 0xf, false);
}
__device__ __forceinline__ unsigned lds_or(int* p, unsigned v) { return __hip_atomic_fetch_or((unsigned*)p, v, __ATOMIC_RELAXED, __HIP_MEMORY_SCOPE_WORKGROUP); }
__device__ __forceinline__ int lds_add(int* p, int v) { return __hip_atomic_fetch_add(p, v, __ATOMIC_RELAXED, __HIP_MEMORY_SCOPE_WORKGROUP); }

__device__ __forceinline__ void dma_tile(__amdgpu_buffer_rsrc_t srd, unsigned voff, unsigned soff, unsigned m0v) {
  const unsigned soff1 = soff + 8u * TOKB;
  asm volatile(
      "s_mov_b32 m0, %0\n\ts_nop 0\n\t"
      "buffer_load_dwordx4 %1, %2, %3 offen nt lds\n\t"
      "s_add_u32 m0, m0, 1024\n\ts_nop 0\n\t"
      "buffer_load_dwordx4 %1, %2, %4 offen nt lds\n\t"
      "s_add_u32 m0, m0, 896\n\ts_nop 0\n\t"
      "buffer_load_dwordx4 %1, %2, %3 offen offset:128 nt lds\n\t"
      "s_add_u32 m0, m0, 1024\n\ts_nop 0\n\t"
      "buffer_load_dwordx4 %1, %2, %4 offen offset:128 nt lds\n\t"
      "s_add_u32 m0, m0, 896\n\ts_nop 0\n\t"
      "buffer_load_dwordx4 %1, %2, %3 offen offset:256 nt lds\n\t"
      "s_add_u32 m0, m0, 1024\n\ts_nop 0\n\t"
      "buffer_load_dwordx4 %1, %2, %4 offen offset:256 nt lds"
      :: "s"(m0v), "v"(voff), "s"(srd), "s"(soff), "s"(soff1) : "memory", "scc");
}
template <int N> __device__ __forceinline__ void wait_vm() { asm volatile("s_waitcnt vmcnt(%0)" :: "i"(N) : "memory"); }
__device__ __forceinline__ void wait_lds() { asm volatile("s_waitcnt lgkmcnt(0)" ::: "memory"); }

__global__ void __launch_bounds__(NTHR, 2) attn_kernel(const __hip_bfloat16* __restrict__ q, const __hip_bfloat16* __restrict__ kc,
                                                       const __hip_bfloat16* __restrict__ vc, const int* __restrict__ bt,
                                                       const int* __restrict__ ctxl, __hip_bfloat16* __restrict__ out,
                                                       unsigned char* __restrict__ ws, int nphys) {
  __shared__ __attribute__((aligned(16))) char lds[LDS_BYTES];
  const int tid = threadIdx.x, lane = tid & 63, wave = __builtin_amdgcn_readfirstlane(tid >> 6);
  const int hg = (blockIdx.x >> 3) & (NHG - 1), s = (blockIdx.x & 7) * (NSPL / 8) + (blockIdx.x >> 6);
  const int hh = wave & 3, pl = wave >> 2;
  const int h = hg * HG + hh;
  const int g = lane >> 4, c16 = lane & 15, cq = lane & 7;
  const int bc = s >> 2, hc = hg * HG + (s & 3);

  const int colid = tid & (NB - 1);
  i32x8 ce; int cbc;
  asm volatile("s_load_dwordx8 %0, %2, 0x0\n\ts_load_dword %1, %2, %3" : "=&s"(ce), "=&s"(cbc) : "s"(ctxl), "s"(bc * 4));
  int key[B];
  u32x4 rowc;
  asm volatile("s_cmp_ge_u32 %13, 4\n\ts_cbranch_scc1 1f\n\t"
               "global_load_dword %1, %9, %10\n\tglobal_load_dword %2, %9, %10 offset:1024\n\tglobal_load_dword %3, %9, %10 offset:2048\n\tglobal_load_dword %4, %9, %10 offset:3072\n\t"
               "global_load_dword %5, %9, %11\n\tglobal_load_dword %6, %9, %11 offset:1024\n\tglobal_load_dword %7, %9, %11 offset:2048\n\tglobal_load_dword %8, %9, %11 offset:3072\n\t"
               "s_branch 2f\n1:\n\tglobal_load_dwordx4 %0, %14, %12\n2:"
               : "=&v"(rowc), "=&v"(key[0]), "=&v"(key[1]), "=&v"(key[2]), "=&v"(key[3]), "=&v"(key[4]), "=&v"(key[5]), "=&v"(key[6]), "=&v"(key[7])
               : "v"(colid * 4), "s"(bt), "s"(bt + 4 * NB), "s"(bt + bc * NB), "s"(wave), "v"(lane * 16) : "scc");
  const unsigned ldsbase = (unsigned)(size_t)(LAS char*)lds;
  const unsigned ring = ldsbase + (unsigned)wave * RINGB;
  {
    const __amdgpu_buffer_rsrc_t srdQ = __builtin_amdgcn_make_buffer_rsrc((void*)q, (short)0, B * H * ROWB, 0x00020000);
    const __amdgpu_buffer_rsrc_t srdE = __builtin_amdgcn_make_buffer_rsrc((void*)(ws + WS_EPOCH), (short)0, H * B * 4, 0x00020000);
    unsigned vo[4];
#pragma unroll
    for (int k = 0; k < 3; ++k) { const unsigned idx = k * 64 + lane, row = (idx * 2731u) >> 16; vo[k] = row * (unsigned)TOKB + (idx - row * 24u) * 16u; }
    vo[3] = (unsigned)(lane - ((lane * 2731) >> 16) * 24) * 16u;
    asm volatile("s_mov_b32 m0, %0\n\ts_nop 0\n\t"
                 "buffer_load_dwordx4 %1, %5, %6 offen lds\n\t"
                 "s_add_u32 m0, m0, 1024\n\ts_nop 0\n\t"
                 "buffer_load_dwordx4 %2, %5, %6 offen lds\n\t"
                 "s_add_u32 m0, m0, 1024\n\ts_nop 0\n\t"
                 "buffer_load_dwordx4 %3, %5, %6 offen lds\n\t"
                 "s_add_u32 m0, m0, 1024\n\ts_nop 0\n\t"
                 "buffer_load_dwordx4 %4, %5, %7 offen lds\n\t"
                 "s_mov_b32 m0, %8\n\ts_nop 0\n\t"
                 "buffer_load_dword %9, %10, %11 offen sc1 lds"
                 :: "s"(ring + 2 * TILEB), "v"(vo[0]), "v"(vo[1]), "v"(vo[2]), "v"(vo[3]), "s"(srdQ), "s"(h * ROWB), "s"((bc * H + hc) * ROWB),
                    "s"(ldsbase + LDS_TAG + wave * 256), "v"((lane & 31) * 4), "s"(srdE), "s"(hg * 128)
                 : "memory", "scc");
  }
  const unsigned kvbytes = (unsigned)nphys * (unsigned)PAGEB;
  const __amdgpu_buffer_rsrc_t srdK = __builtin_amdgcn_make_buffer_rsrc((void*)kc, (short)0, (int)kvbytes, 0x00020000);
  const __amdgpu_buffer_rsrc_t srdV = __builtin_amdgcn_make_buffer_rsrc((void*)vc, (short)0, (int)kvbytes, 0x00020000);
  const unsigned voff = (unsigned)(lane >> 3) * TOKB + (unsigned)(((lane & 7) ^ ((lane >> 3) & 6)) * 16);
  const unsigned hoff = (unsigned)h * ROWB;
  int* cnt = reinterpret_cast<int*>(lds + LDS_CNT);
  asm volatile("s_waitcnt lgkmcnt(0)" : "+s"(ce), "+s"(cbc) :: "memory");
  const int npb = (((cbc <= 0 || cbc > LK) ? LK : cbc) + BS - 1) / BS;
  const int colp = npb - NPRE + (wave - 4);
  const bool vP = wave >= 4 && colp >= 0;
  int phA = 0, mtA = (colp & 0xffff) | (1 << (16 + bc));
  unsigned long long bal[B];
  asm volatile("s_waitcnt vmcnt(5)" : "+v"(rowc), "+v"(key[0]), "+v"(key[1]), "+v"(key[2]), "+v"(key[3]), "+v"(key[4]), "+v"(key[5]), "+v"(key[6]), "+v"(key[7]) :: "memory");
  if (wave >= 4) {
    const int x01 = (int)((colp & 1) ? rowc[1] : rowc[0]), x23 = (int)((colp & 1) ? rowc[3] : rowc[2]);
    const int p = __builtin_amdgcn_readlane((colp & 2) ? x23 : x01, (colp >> 2) & 63);
    phA = p + ((p >> 31) & nphys);
    if (vP) {
      dma_tile(srdK, voff, (unsigned)phA * (unsigned)PAGEB + (unsigned)hc * ROWB, ring);
      dma_tile(srdV, voff, (unsigned)phA * (unsigned)PAGEB + (unsigned)hc * ROWB, ring + TILEB);
    }
#pragma unroll
    for (int b = 0; b < B; ++b) { bal[b] = 0ull; key[b] = -1; }
  } else {
    const int c0 = (colid + NPRE) * BS;
#pragma unroll
    for (int b = 0; b < B; ++b) {
      const int c = (ce[b] <= 0 || ce[b] > LK) ? LK : ce[b];
      const int p = key[b] + ((key[b] >> 31) & nphys);
      key[b] = (c0 < c) ? p : -1;
    }
    unsigned blo = 0, bhi = 0;
#pragma unroll
    for (int b = 0; b < B; ++b) {
      unsigned dm = (unsigned)(key[b] + 1);
#pragma unroll
      for (int b2 = 0; b2 < b; ++b2) dm = min(dm, (unsigned)(key[b] ^ key[b2]));
      bal[b] = __ballot(dm != 0u);
      asm("s_nop 1\n\tv_writelane_b32 %0, %2, %4\n\tv_writelane_b32 %1, %3, %4"
          : "+v"(blo), "+v"(bhi) : "s"((unsigned)bal[b]), "s"((unsigned)(bal[b] >> 32)), "i"(b));
    }
    if (lane < B) *reinterpret_cast<u32x2*>(lds + LDS_BAL + (lane * 4 + wave) * 8) = u32x2{blo, bhi};
    if (tid < 12) cnt[tid] = (tid >= 4 && tid < 8) ? 3 : 0;
  }
  wait_lds();
  __builtin_amdgcn_s_barrier();
  const u32x2 gb = *reinterpret_cast<const u32x2*>(lds + LDS_BAL + (lane & 31) * 8);
  const int gown = __popc(gb[0]) + __popc(gb[1]);
  int incl = gown;
  incl = dpp_add<0x111>(incl); incl = dpp_add<0x112>(incl); incl = dpp_add<0x114>(incl); incl = dpp_add<0x118>(incl);
  incl = dpp_add<0x142, 0xa>(incl);
  const int excl = incl - gown;
  const int ntot = __builtin_amdgcn_readlane(incl, 31);
  const int lo = (int)(((unsigned)ntot * (unsigned)s) / NSPL), hi = (int)(((unsigned)ntot * (unsigned)(s + 1)) / NSPL);
  const int nloc = hi - lo;
  if (wave < 4) {
    int pos[B];
#pragma unroll
    for (int b = 0; b < B; ++b) {
      pos[b] = __builtin_amdgcn_readlane(excl, b * 4 + wave) + (int)__builtin_amdgcn_mbcnt_hi((unsigned)(bal[b] >> 32), __builtin_amdgcn_mbcnt_lo((unsigned)bal[b], 0u));
      const int rel = pos[b] - lo;
      if (((bal[b] >> lane) & 1ull) && (unsigned)rel < (unsigned)nloc) {
        int mk = 0;
#pragma unroll
        for (int b2 = b; b2 < B; ++b2) mk |= (key[b2] == key[b]) ? (1 << (16 + b2)) : 0;
        *reinterpret_cast<int2*>(lds + LDS_LIST + rel * 8) = make_int2(key[b], colid | mk);
      }
    }
    int kc_ = key[0];
#pragma unroll
    for (int b = 1; b < B; ++b) if (b == bc) kc_ = key[b];
    int mp = 0;
#pragma unroll
    for (int b = B - 1; b >= 0; --b) if (key[b] == kc_) mp = pos[b];
    unsigned bit = 0;
    if (kc_ >= 0) {
      const int x = NSPL * mp + NSPL - 1;
      int sp = (int)((float)x * (1.0f / (float)ntot));
      const int r = x - sp * ntot;
      sp += (r >= ntot) ? 1 : 0; sp -= (r < 0) ? 1 : 0;
      bit = 1u << sp;
    }
    bit |= __builtin_amdgcn_update_dpp(0, bit, 0x111, 0xf, 0xf, false);
    bit |= __builtin_amdgcn_update_dpp(0, bit, 0x112, 0xf, 0xf, false);
    bit |= __builtin_amdgcn_update_dpp(0, bit, 0x114, 0xf, 0xf, false);
    bit |= __builtin_amdgcn_update_dpp(0, bit, 0x118, 0xf, 0xf, false);
    const unsigned all = __builtin_amdgcn_readlane(bit, 15) | __builtin_amdgcn_readlane(bit, 31) | __builtin_amdgcn_readlane(bit, 47) | __builtin_amdgcn_readlane(bit, 63);
    if (lane == 0) lds_or(cnt, all);
  }
  wait_lds();
  __builtin_amdgcn_s_barrier();
  bool vA, vB;
  int phB, mtB;
  bool first = vP;
  u32x4 qf[6], qm[6];
  {
    const int2 e0 = *reinterpret_cast<const int2*>(lds + LDS_LIST + pl * 8), e2 = *reinterpret_cast<const int2*>(lds + LDS_LIST + 2 * 8);
    const int p0 = __builtin_amdgcn_readfirstlane(e0.x), m0 = __builtin_amdgcn_readfirstlane(e0.y);
    if (vP) {
      vA = true; vB = 1 < nloc; phB = p0; mtB = m0;
    } else {
      vA = pl < nloc; phA = p0; mtA = m0;
      if (vA) {
        dma_tile(srdK, voff, (unsigned)phA * (unsigned)PAGEB + hoff, ring);
        dma_tile(srdV, voff, (unsigned)phA * (unsigned)PAGEB + hoff, ring + TILEB);
      }
      if (pl == 0) {
        vB = 2 < nloc; phB = __builtin_amdgcn_readfirstlane(e2.x); mtB = __builtin_amdgcn_readfirstlane(e2.y);
      } else {
        int idx = 64;
        if (vA && lane == 0) idx = lds_add(cnt + 4 + hh, 1);
        idx = __builtin_amdgcn_readfirstlane(idx);
        vB = vA && idx < nloc;
        const int2 e1 = *reinterpret_cast<const int2*>(lds + LDS_LIST + (idx & 63) * 8);
        phB = __builtin_amdgcn_readfirstlane(e1.x); mtB = __builtin_amdgcn_readfirstlane(e1.y);
      }
    }
    if (vA) wait_vm<13>(); else wait_vm<0>();
    const char* qs = lds + wave * RINGB + 2 * TILEB;
#pragma unroll
    for (int i = 0; i < 6; ++i) {
      qm[i] = *reinterpret_cast<const u32x4*>(qs + cq * ROWB + i * 64 + g * 16);
      qf[i] = *reinterpret_cast<const u32x4*>(qs + (vP ? B * ROWB : cq * ROWB) + i * 64 + g * 16);
    }
    asm volatile("s_waitcnt lgkmcnt(0)" : "+v"(qf[0]), "+v"(qf[1]), "+v"(qf[2]), "+v"(qf[3]), "+v"(qf[4]), "+v"(qf[5]),
                 "+v"(qm[0]), "+v"(qm[1]), "+v"(qm[2]), "+v"(qm[3]), "+v"(qm[4]), "+v"(qm[5]) :: "memory");
    if (vB) dma_tile(srdK, voff, (unsigned)phB * (unsigned)PAGEB + hoff, ring + 2 * TILEB);
  }
  int myctx_raw = ce[0];
#pragma unroll
  for (int b = 1; b < B; ++b) myctx_raw = (cq == b) ? ce[b] : myctx_raw;
  const int myctx = (myctx_raw <= 0 || myctx_raw > LK) ? LK : myctx_raw;
  {
    const bool zq = (c16 >= B) || (myctx_raw <= 0);
    const bool zf = vP ? (cbc <= 0) : zq;
#pragma unroll
    for (int i = 0; i < 6; ++i) { if (zq) qm[i] = u32x4{0u, 0u, 0u, 0u}; if (zf) qf[i] = u32x4{0u, 0u, 0u, 0u}; }
  }
  const unsigned* etag = reinterpret_cast<const unsigned*>(lds + LDS_TAG + wave * 256);

  const __amdgpu_buffer_rsrc_t srdF = __builtin_amdgcn_make_buffer_rsrc((void*)(ws + WS_FLAG), (short)0, (int)(WS_REC - WS_FLAG), 0x00020000);
  unsigned kaddr[2], vaddr[4];
#pragma unroll
  for (int bb = 0; bb < 2; ++bb) kaddr[bb] = ring + c16 * 128 + (((4 * bb + g) ^ (c16 & 6)) * 16);
  {
    const int t = 4 * g + (c16 >> 2), p = lane & 3;
#pragma unroll
    for (int k = 0; k < 4; ++k) vaddr[k] = ring + t * 128 + ((((2 * k) ^ (t & 6)) + (p >> 1)) * 16) + (p & 1) * 8;
  }
  float m = -INFINITY, l = 0.f;
  f32x4 acc[12];
#pragma unroll
  for (int i = 0; i < 12; ++i) acc[i] = f32x4{0.f, 0.f, 0.f, 0.f};

  unsigned seen = 0;
  auto early = [&]() -> bool {
    unsigned old = 0;
    if (lane == 0) old = lds_or(cnt + 8 + hh, seen | (1u << pl));
    old = __builtin_amdgcn_readfirstlane(old);
    const unsigned wm = (old | seen) >> 16;
    const bool go = ((old >> (1 - pl)) & 1u) && wm != 0u;
    if (go) {
      const unsigned mytag = etag[hh * 8 + cq] + 1u;
      if (lane < B && ((wm >> lane) & 1u)) __builtin_amdgcn_raw_buffer_store_b32(mytag, srdF, ((h * B + lane) * NSPL + s) * 4, 0, 16);
    }
    return go;
  };

  unsigned slot = 0;
  const bool nopages = !vA;
  while (vA) {
    int vidx = 0;
    if (vB && lane == 0) vidx = lds_add(cnt + 4 + hh, 1);
    seen |= first ? 0u : ((unsigned)mtA & 0xff0000u);
    if (vB) wait_vm<12>(); else wait_vm<6>();
    bf16x8 kf[6];
#pragma unroll
    for (int u = 0; u < 6; ++u) kf[u] = *(const LAS bf16x8*)(size_t)(kaddr[u & 1] + slot + (u >> 1) * 2048);
    asm volatile("s_waitcnt lgkmcnt(0)" : "+v"(kf[0]), "+v"(kf[1]), "+v"(kf[2]), "+v"(kf[3]), "+v"(kf[4]), "+v"(kf[5]), "+v"(vidx) :: "memory");
    if (vB) dma_tile(srdV, voff, (unsigned)phB * (unsigned)PAGEB + hoff, ring + slot);
    const int idxC = __builtin_amdgcn_readfirstlane(vidx);
    const bool vC = vB && idxC < nloc;
    int2 ec = *reinterpret_cast<const int2*>(lds + LDS_LIST + (idxC & 63) * 8);
    slot = (slot + TILEB == RINGB) ? 0u : slot + TILEB;
    f32x4 s0 = {0.f, 0.f, 0.f, 0.f}, s1 = {0.f, 0.f, 0.f, 0.f};
#pragma unroll
    for (int u = 0; u < 6; u += 2) {
      s0 = __builtin_amdgcn_mfma_f32_16x16x32_bf16(kf[u], __builtin_bit_cast(bf16x8, qf[u]), s0, 0, 0, 0);
      s1 = __builtin_amdgcn_mfma_f32_16x16x32_bf16(kf[u + 1], __builtin_bit_cast(bf16x8, qf[u + 1]), s1, 0, 0, 0);
    }
    const int nval = ((mtA >> (16 + c16)) & 1) ? (myctx - (mtA & 0xffff) * BS - 4 * g) : 0;
    float sc[4];
#pragma unroll
    for (int r = 0; r < 4; ++r) sc[r] = (r < nval) ? (s0[r] + s1[r]) * SCALE_LOG2E : -INFINITY;
    float tm = fmaxf(fmaxf(sc[0], sc[1]), fmaxf(sc[2], sc[3]));
    tm = xrow16_max(tm);
    const float mn = fmaxf(m, tm);
    const float msub = (mn == -INFINITY) ? 0.f : mn;
    const float alpha = __builtin_amdgcn_exp2f(m - msub);
    float p[4];
#pragma unroll
    for (int r = 0; r < 4; ++r) p[r] = __builtin_amdgcn_exp2f(sc[r] - msub);
    l = l * alpha + ((p[0] + p[1]) + (p[2] + p[3]));
    if (__any(mn > m)) {
#pragma unroll
      for (int dt = 0; dt < 12; ++dt) acc[dt] *= alpha;
    }
    m = mn;
    const bf16x4 pb4 = {(__bf16)p[0], (__bf16)p[1], (__bf16)p[2], (__bf16)p[3]};
    const s16x4 pf = __builtin_bit_cast(s16x4, pb4);
    if (vB) wait_vm<12>();
    else { wait_vm<0>(); early(); }
    s16x4 vf[12];
#pragma unroll
    for (int dt = 0; dt < 12; ++dt)
      vf[dt] = __builtin_amdgcn_ds_read_tr16_b64_v4i16((LAS s16x4*)(size_t)(vaddr[dt & 3] + slot + (dt >> 2) * 2048));
    asm volatile("s_waitcnt lgkmcnt(0)"
                 : "+v"(vf[0]), "+v"(vf[1]), "+v"(vf[2]), "+v"(vf[3]), "+v"(vf[4]), "+v"(vf[5]), "+v"(vf[6]), "+v"(vf[7]), "+v"(vf[8]), "+v"(vf[9]), "+v"(vf[10]), "+v"(vf[11]),
                   "+v"(ec.x), "+v"(ec.y)
                 :: "memory");
    const int phC = __builtin_amdgcn_readfirstlane(ec.x), mtC = __builtin_amdgcn_readfirstlane(ec.y);
    if (vC) dma_tile(srdK, voff, (unsigned)phC * (unsigned)PAGEB + hoff, ring + slot);
    slot = (slot + TILEB == RINGB) ? 0u : slot + TILEB;
#pragma unroll
    for (int dt = 0; dt < 12; ++dt) acc[dt] = __builtin_amdgcn_mfma_f32_16x16x16bf16_1k(vf[dt], pf, acc[dt], 0, 0, 0);
    if (first) {
      first = false;
      const float ls = xrow16_sum(l);
      float* xp = reinterpret_cast<float*>(lds + LDS_PRE) + (wave - 4) * PROW;
      if (c16 == bc) {
#pragma unroll
        for (int dt = 0; dt < 12; ++dt) *reinterpret_cast<f32x4*>(xp + 16 * dt + 4 * g) = acc[dt];
        if (g == 0) *reinterpret_cast<f32x2*>(xp + D) = f32x2{m, ls};
      }
      wait_lds();
      if (lane == 0) lds_add(cnt + 2, 1);
      m = -INFINITY; l = 0.f;
#pragma unroll
      for (int dt = 0; dt < 12; ++dt) acc[dt] = f32x4{0.f, 0.f, 0.f, 0.f};
#pragma unroll
      for (int i = 0; i < 6; ++i) qf[i] = qm[i];
    }
    phA = phB; mtA = mtB; vA = vB;
    phB = phC; mtB = mtC; vB = vC;
  }
  if (nopages) early();

  const __amdgpu_buffer_rsrc_t srdR = __builtin_amdgcn_make_buffer_rsrc((void*)(ws + WS_REC), (short)0, (int)(WS_END - WS_REC), 0x00020000);
  unsigned pw = 0;
  if (lane == 0) pw = lds_or(cnt + 8 + hh, (4u << pl) | seen);
  l = xrow16_sum(l);
  auto from8 = [](float lo, float hi) -> float {
    return __int_as_float(__builtin_amdgcn_update_dpp(__float_as_int(lo), __float_as_int(hi), 0x118, 0xf, 0xc, false));
  };
  f32x4 xa[6];
#pragma unroll
  for (int u = 0; u < 6; ++u)
#pragma unroll
    for (int r = 0; r < 4; ++r) xa[u][r] = from8(acc[u][r], acc[6 + u][r]);
  const float ma = from8(m, m), la = from8(l, l);
  const int xoff = (c16 >> 3) * 6 * 512 + (g * 8 + cq) * 16;
  pw = __builtin_amdgcn_readfirstlane(pw);
  if (!((pw >> (3 - pl)) & 1u)) {
    char* xp = lds + wave * RINGB;
#pragma unroll
    for (int u = 0; u < 6; ++u) *reinterpret_cast<f32x4*>(xp + xoff + u * 512) = xa[u];
    if (lane < B) *reinterpret_cast<f32x2*>(xp + 6144 + lane * 8) = f32x2{ma, la};
    wait_lds();
    if (lane == 0) lds_or(cnt + 8 + hh, 16u << pl);
  } else {
    if (!((pw >> (5 - pl)) & 1u)) {
      while (!((__hip_atomic_load(cnt + 8 + hh, __ATOMIC_RELAXED, __HIP_MEMORY_SCOPE_WORKGROUP) >> (5 - pl)) & 1)) __builtin_amdgcn_s_sleep(1);
    }
    asm volatile("" ::: "memory");
    pw |= seen;
    const char* xp = lds + (wave ^ 4) * RINGB;
    f32x4 xb[6];
#pragma unroll
    for (int u = 0; u < 6; ++u) xb[u] = *reinterpret_cast<const f32x4*>(xp + xoff + u * 512);
    const f32x2 mlb = *reinterpret_cast<const f32x2*>(xp + 6144 + cq * 8);
    const unsigned tag = etag[hh * 8 + cq] + 1u;
    const float M = fmaxf(ma, mlb[0]);
    const float wa = la > 0.f ? __builtin_amdgcn_exp2f(ma - M) : 0.f, wb = mlb[1] > 0.f ? __builtin_amdgcn_exp2f(mlb[0] - M) : 0.f;
    const float L = wa * la + wb * mlb[1];
    const float inv = __builtin_amdgcn_rcpf(L);
    const unsigned lse = __float_as_uint(M + __builtin_amdgcn_logf(L));
    const unsigned tw = (tag << 8) | ((lse >> (8 * g)) & 0xffu);
    const float sa = wa * inv, sb = wb * inv;
    float y[24];
#pragma unroll
    for (int t = 0; t < 24; ++t) y[t] = sa * xa[t >> 2][t & 3] + sb * xb[t >> 2][t & 3];
    if ((pw >> (16 + cq)) & 1u) {
      const int ro = ((h * B + cq) * NSPL + s) * RECB + ((c16 >> 3) * 4 + g) * 16;
#pragma unroll
      for (int jj = 0; jj < 8; ++jj)
        __builtin_amdgcn_raw_buffer_store_b128(u32x4{__float_as_uint(y[3 * jj]), __float_as_uint(y[3 * jj + 1]), __float_as_uint(y[3 * jj + 2]), tw}, srdR, ro + jj * 128, 0, 16);
    }
    return;
  }
  {
    int tk = 0;
    if (lane == 0) tk = lds_add(cnt + 1, 1);
    if (__builtin_amdgcn_readfirstlane(tk) != 0) return;
  }
  {
    const unsigned touch = (unsigned)__builtin_amdgcn_readfirstlane(cnt[0]);
    const unsigned ct = (unsigned)__builtin_amdgcn_readfirstlane((int)etag[(s & 3) * 8 + bc]) + 1u;
    const unsigned ct24 = ct & 0xffffffu;
    int dk[3];
#pragma unroll
    for (int k = 0; k < 3; ++k) { const int v = 24 * ((lane >> 2) & 1) + 3 * (lane >> 3) + k; dk[k] = 16 * (v >> 2) + 4 * (lane & 3) + (v & 3); }
    float M = -INFINITY, L = 0.f, o[3] = {0.f, 0.f, 0.f};
    unsigned pend = touch, armed = 0;
    const unsigned long long t0 = __builtin_amdgcn_s_memrealtime();
    {
      const int nv = min(npb, NPRE);
      while (__hip_atomic_load(cnt + 2, __ATOMIC_RELAXED, __HIP_MEMORY_SCOPE_WORKGROUP) < nv) __builtin_amdgcn_s_sleep(1);
      asm volatile("" ::: "memory");
      const float* xp = reinterpret_cast<const float*>(lds + LDS_PRE);
      f32x2 ml[NPRE]; float x[NPRE][3];
#pragma unroll
      for (int w = 0; w < NPRE; ++w) {
        ml[w] = *reinterpret_cast<const f32x2*>(xp + w * PROW + D);
#pragma unroll
        for (int j = 0; j < 3; ++j) x[w][j] = xp[w * PROW + dk[j]];
      }
#pragma unroll
      for (int w = 0; w < NPRE; ++w) if (w >= NPRE - nv) M = fmaxf(M, ml[w][0]);
#pragma unroll
      for (int w = 0; w < NPRE; ++w) {
        if (w >= NPRE - nv) {
          const float wt = __builtin_amdgcn_exp2f(ml[w][0] - M);
          L += wt * ml[w][1];
#pragma unroll
          for (int j = 0; j < 3; ++j) o[j] += wt * x[w][j];
        }
      }
    }
    const int recbase = (hc * B + bc) * NSPL;
    while (pend) {
      asm volatile("" ::: "memory");
      unsigned fl = ct;
      if ((pend & ~armed) && lane < 32) fl = __builtin_amdgcn_raw_buffer_load_b32(srdF, (recbase + lane) * 4, 0, 16);
      int sp[8]; u32x4 a[8];
      unsigned rest = pend & armed;
#pragma unroll
      for (int r = 0; r < 8; ++r) {
        sp[r] = rest ? __builtin_ctz(rest) : -1; rest &= rest - 1u;
        if (sp[r] >= 0) a[r] = __builtin_amdgcn_raw_buffer_load_b128(srdR, (recbase + sp[r]) * RECB + lane * 16, 0, 16);
        else a[r] = u32x4{0u, 0u, 0u, 0u};
      }
      unsigned done = 0;
#pragma unroll
      for (int r = 0; r < 8; ++r) {
        if (sp[r] >= 0) {
          if (__all((a[r][3] >> 8) == ct24)) {
            done |= 1u << sp[r];
            unsigned x = (a[r][3] & 0xffu) << (8 * (lane & 3));
            x |= __builtin_amdgcn_update_dpp(0u, x, 0xb1, 0xf, 0xf, false);
            x |= __builtin_amdgcn_update_dpp(0u, x, 0x4e, 0xf, 0xf, false);
            const float lse = __uint_as_float(x);
            const float Mn = fmaxf(M, lse);
            const float wo = __builtin_amdgcn_exp2f(M - Mn), wn = __builtin_amdgcn_exp2f(lse - Mn);
#pragma unroll
            for (int j = 0; j < 3; ++j) o[j] = wo * o[j] + wn * __uint_as_float(a[r][j]);
            L = wo * L + wn; M = Mn;
          }
        }
      }
      pend &= ~done;
      const unsigned was = armed;
      armed |= (unsigned)__ballot(lane < 32 && fl == ct);
      if (!done && pend) {
        if (__builtin_amdgcn_s_memrealtime() - t0 > 200000ull) break;
        if (pend & was) __builtin_amdgcn_s_sleep(1); else if (!(pend & armed)) __builtin_amdgcn_s_sleep(8);
      }
    }
    const float inv = 1.f / L;
    __hip_bfloat16* orow = out + ((size_t)bc * H + hc) * D;
#pragma unroll
    for (int j = 0; j < 3; ++j) *reinterpret_cast<__bf16*>(orow + dk[j]) = (__bf16)(o[j] * inv);
    if (lane == 0) __hip_atomic_store((GAS unsigned*)(ws + WS_EPOCH) + hc * B + bc, ct, __ATOMIC_RELAXED, __HIP_MEMORY_SCOPE_AGENT);
  }
}

extern "C" void kernel_launch(void* const* d_in, const int* in_sizes, int n_in, void* d_out, int out_size, void* d_ws, size_t ws_size,
                              hipStream_t stream) {
  const __hip_bfloat16* q = (const __hip_bfloat16*)d_in[0];
  const __hip_bfloat16* k = (const __hip_bfloat16*)d_in[1];
  const __hip_bfloat16* v = (const __hip_bfloat16*)d_in[2];
  const int* bt = (const int*)d_in[3];
  const int* ctx = (const int*)d_in[4];
  const int nphys = in_sizes[1] / (BS * H * D);
  attn_kernel<<<NWG, NTHR, 0, stream>>>(q, k, v, bt, ctx, (__hip_bfloat16*)d_out, (unsigned char*)d_ws, nphys);
}
```

```cpp
#include <hip/hip_runtime.h>
#include <hip/hip_bf16.h>
#include <cstdint>

constexpr int B = 8, H = 32, D = 192, NB = 256, BS = 16, LK = NB * BS;
constexpr int ROWB = D * 2;
constexpr int TOKB = H * ROWB;
constexpr int PAGEB = BS * TOKB;
constexpr int W = 8, HG = 4, NHG = H / HG, NSPL = 32, NWG = NHG * NSPL, NTHR = W * 64;
constexpr int NS = 3;
constexpr int TILEB = BS * ROWB;
constexpr int RINGB = NS * TILEB;
constexpr int PROW = 200;
constexpr int NPRE = 4;
constexpr int NOWN = 8;
constexpr int LDS_PRE = W * RINGB;
constexpr int NLIST = 128;
constexpr int LDS_LIST = LDS_PRE + NOWN * PROW * 4;
constexpr int LDS_BAL = LDS_LIST + NLIST * 8;
constexpr int LDS_CNT = LDS_BAL + 32 * 8;
constexpr int LDS_TAG = LDS_CNT + 16 * 4;
constexpr int LDS_BYTES = LDS_TAG + W * 256;
constexpr int RECB = 512;
constexpr size_t WS_EPOCH = 0, WS_CTL = 4096, WS_REC = WS_CTL + (size_t)NWG * 128, WS_END = WS_REC + (size_t)H * B * NSPL * RECB;
constexpr float SCALE_LOG2E = 0.07216878364870322f * 1.4426950408889634f;

#define LAS __attribute__((address_space(3)))
typedef __attribute__((ext_vector_type(8))) __bf16 bf16x8;
typedef __attribute__((ext_vector_type(4))) short s16x4;
typedef __attribute__((ext_vector_type(4))) float f32x4;
typedef __attribute__((ext_vector_type(2))) float f32x2;
typedef __attribute__((ext_vector_type(4))) unsigned u32x4;
typedef __attribute__((ext_vector_type(2))) unsigned u32x2;
typedef __attribute__((ext_vector_type(8))) int i32x8;
typedef __attribute__((ext_vector_type(2))) _Float16 f16x2;
typedef __attribute__((ext_vector_type(2))) __bf16 bf16x2;

__device__ __forceinline__ float xrow16_max(float x) {
  auto s = __builtin_amdgcn_permlane16_swap(__float_as_uint(x), __float_as_uint(x), false, false);
  x = fmaxf(__uint_as_float(s[0]), __uint_as_float(s[1]));
  auto t = __builtin_amdgcn_permlane32_swap(__float_as_uint(x), __float_as_uint(x), false, false);
  return fmaxf(__uint_as_float(t[0]), __uint_as_float(t[1]));
}
__device__ __forceinline__ float xrow16_sum(float x) {
  auto s = __builtin_amdgcn_permlane16_swap(__float_as_uint(x), __float_as_uint(x), false, false);
  x = __uint_as_float(s[0]) + __uint_as_float(s[1]);
  auto t = __builtin_amdgcn_permlane32_swap(__float_as_uint(x), __float_as_uint(x), false, false);
  return __uint_as_float(t[0]) + __uint_as_float(t[1]);
}
template <int CTRL, int RM = 0xf> __device__ __forceinline__ int dpp_add(int x) {
  return x + __builtin_amdgcn_update_dpp(0, x, CTRL, RM, 0xf, false);
}
__device__ __forceinline__ unsigned lds_or(int* p, unsigned v) { return __hip_atomic_fetch_or((unsigned*)p, v, __ATOMIC_RELAXED, __HIP_MEMORY_SCOPE_WORKGROUP); }
__device__ __forceinline__ int lds_add(int* p, int v) { return __hip_atomic_fetch_add(p, v, __ATOMIC_RELAXED, __HIP_MEMORY_SCOPE_WORKGROUP); }

__device__ __forceinline__ void dma_tile(__amdgpu_buffer_rsrc_t srd, unsigned voff, unsigned soff, unsigned m0v) {
  const unsigned soff1 = soff + 8u * TOKB;
  asm volatile(
      "s_mov_b32 m0, %0\n\ts_nop 0\n\t"
      "buffer_load_dwordx4 %1, %2, %3 offen nt lds\n\t"
      "s_add_u32 m0, m0, 1024\n\ts_nop 0\n\t"
      "buffer_load_dwordx4 %1, %2, %4 offen nt lds\n\t"
      "s_add_u32 m0, m0, 896\n\ts_nop 0\n\t"
      "buffer_load_dwordx4 %1, %2, %3 offen offset:128 nt lds\n\t"
      "s_add_u32 m0, m0, 1024\n\ts_nop 0\n\t"
      "buffer_load_dwordx4 %1, %2, %4 offen offset:128 nt lds\n\t"
      "s_add_u32 m0, m0, 896\n\ts_nop 0\n\t"
      "buffer_load_dwordx4 %1, %2, %3 offen offset:256 nt lds\n\t"
      "s_add_u32 m0, m0, 1024\n\ts_nop 0\n\t"
      "buffer_load_dwordx4 %1, %2, %4 offen offset:256 nt lds"
      :: "s"(m0v), "v"(voff), "s"(srd), "s"(soff), "s"(soff1) : "memory", "scc");
}
template <int N> __device__ __forceinline__ void wait_vm() { asm volatile("s_waitcnt vmcnt(%0)" :: "i"(N) : "memory"); }
__device__ __forceinline__ void wait_lds() { asm volatile("s_waitcnt lgkmcnt(0)" ::: "memory"); }

__global__ void __launch_bounds__(NTHR, 2) attn_kernel(const __hip_bfloat16* __restrict__ q, const __hip_bfloat16* __restrict__ kc,
                                                       const __hip_bfloat16* __restrict__ vc, const int* __restrict__ bt,
                                                       const int* __restrict__ ctxl, __hip_bfloat16* __restrict__ out,
                                                       unsigned char* __restrict__ ws, int nphys) {
  asm volatile("" :: "s"(q), "s"(kc), "s"(vc), "s"(bt), "s"(ctxl), "s"(out), "s"(ws), "s"(nphys));
  __shared__ __attribute__((aligned(16))) char lds[LDS_BYTES];
  const int tid = threadIdx.x, lane = tid & 63, wave = __builtin_amdgcn_readfirstlane(tid >> 6);
  const int hg = ((blockIdx.x >> 3) + blockIdx.x) & (NHG - 1), s = ((blockIdx.x & 6) << 2) + (blockIdx.x >> 6) * 2 + (blockIdx.x & 1);
  const int hh = wave & 3, pl = wave >> 2;
  const int h = hg * HG + hh;
  const int g = lane >> 4, c16 = lane & 15, cq = lane & 7;
  const int bc = s >> 2, hc = hg * HG + (s & 3);

  const int colid = tid & (NB - 1);
  i32x8 ce; int cbc;
  asm volatile("s_load_dwordx8 %0, %2, 0x0\n\ts_load_dword %1, %2, %3" : "=&s"(ce), "=&s"(cbc) : "s"(ctxl), "s"(bc * 4));
  int key[B];
  u32x4 rowc;
  asm volatile("s_cmp_ge_u32 %13, 4\n\ts_cbranch_scc1 1f\n\t"
               "global_load_dword %1, %9, %10\n\tglobal_load_dword %2, %9, %10 offset:1024\n\tglobal_load_dword %3, %9, %10 offset:2048\n\tglobal_load_dword %4, %9, %10 offset:3072\n\t"
               "global_load_dword %5, %9, %11\n\tglobal_load_dword %6, %9, %11 offset:1024\n\tglobal_load_dword %7, %9, %11 offset:2048\n\tglobal_load_dword %8, %9, %11 offset:3072\n\t"
               "s_branch 2f\n1:\n\tglobal_load_dwordx4 %0, %14, %12\n2:"
               : "=&v"(rowc), "=&v"(key[0]), "=&v"(key[1]), "=&v"(key[2]), "=&v"(key[3]), "=&v"(key[4]), "=&v"(key[5]), "=&v"(key[6]), "=&v"(key[7])
               : "v"(colid * 4), "s"(bt), "s"(bt + 4 * NB), "s"(bt + bc * NB), "s"(wave), "v"(lane * 16) : "scc");
  const unsigned ldsbase = (unsigned)(size_t)(LAS char*)lds;
  const unsigned ring = ldsbase + (unsigned)wave * RINGB;
  {
    const __amdgpu_buffer_rsrc_t srdQ = __builtin_amdgcn_make_buffer_rsrc((void*)q, (short)0, B * H * ROWB, 0x00020000);
    typedef unsigned long long u64;
    const unsigned piece = (blockIdx.x >> 3) * 4 + (wave & 3);
    const u64 far = (u64)(wave < 4 ? kc : vc) + ((u64)(piece < (((unsigned)nphys * 3u) >> 5) ? piece : 0u) << 21);
    u64 a1 = (u64)out + (unsigned)((bc * H + hc) * ROWB);
    unsigned a2 = (unsigned)((h * B * NSPL + s) * RECB), a3 = (unsigned)((hc * B + bc) * NSPL * RECB);
    asm("" : "+s"(a1), "+s"(a2), "+s"(a3));
    const u64 a4 = (u64)ws + (WS_REC + ((wave & 2) ? a2 : a3));
    const u64 far2 = (wave & 1) ? a1 : a4;
    const u64 eb = (u64)ws + (WS_EPOCH + (unsigned)(hg * 128));
    u32x2 eadr;
    asm("v_lshl_add_u32 %0, %2, 2, %3\n\tv_mov_b32 %1, %4\n\t"
        "s_mov_b64 exec, %9\n\tv_mov_b32 %0, %5\n\tv_mov_b32 %1, %6\n\t"
        "s_mov_b64 exec, %10\n\tv_mov_b32 %0, %7\n\tv_mov_b32 %1, %8\n\t"
        "s_mov_b64 exec, -1"
        : "=&v"(eadr[0]), "=&v"(eadr[1])
        : "v"(lane & 31), "s"((unsigned)eb), "s"((unsigned)(eb >> 32)), "s"((unsigned)far), "s"((unsigned)(far >> 32)),
          "s"((unsigned)far2), "s"((unsigned)(far2 >> 32)),
          "s"(2 ? 0x0000ffff00000000ull : 0ull), "s"(2 > 1 ? 0xffff000000000000ull : 2 ? 0ull : 0ull));
    unsigned vo[4];
#pragma unroll
    for (int k = 0; k < 3; ++k) { const unsigned idx = k * 64 + lane, row = (idx * 2731u) >> 16; vo[k] = row * (unsigned)TOKB + (idx - row * 24u) * 16u; }
    vo[3] = (unsigned)(lane - ((lane * 2731) >> 16) * 24) * 16u;
    asm volatile("s_mov_b32 m0, %0\n\ts_nop 0\n\t"
                 "buffer_load_dwordx4 %1, %5, %6 offen lds\n\t"
                 "s_add_u32 m0, m0, 1024\n\ts_nop 0\n\t"
                 "buffer_load_dwordx4 %2, %5, %6 offen lds\n\t"
                 "s_add_u32 m0, m0, 1024\n\ts_nop 0\n\t"
                 "buffer_load_dwordx4 %3, %5, %6 offen lds\n\t"
                 "s_add_u32 m0, m0, 1024\n\ts_nop 0\n\t"
                 "buffer_load_dwordx4 %4, %5, %7 offen lds\n\t"
                 "s_mov_b32 m0, %8\n\ts_nop 0\n\t"
                 "global_load_lds_dword %9, off sc1"
                 :: "s"(ring + 2 * TILEB), "v"(vo[0]), "v"(vo[1]), "v"(vo[2]), "v"(vo[3]), "s"(srdQ), "s"(h * ROWB), "s"((bc * H + hc) * ROWB),
                    "s"(ldsbase + LDS_TAG + wave * 256), "v"(eadr)
                 : "memory", "scc");
  }
  const unsigned kvbytes = (unsigned)nphys * (unsigned)PAGEB;
  const __amdgpu_buffer_rsrc_t srdK = __builtin_amdgcn_make_buffer_rsrc((void*)kc, (short)0, (int)kvbytes, 0x00020000);
  const __amdgpu_buffer_rsrc_t srdV = __builtin_amdgcn_make_buffer_rsrc((void*)vc, (short)0, (int)kvbytes, 0x00020000);
  const unsigned voff = (unsigned)(lane >> 3) * TOKB + (unsigned)(((lane & 7) ^ ((lane >> 3) & 6)) * 16);
  const unsigned hoff = (unsigned)h * ROWB;
  int* cnt = reinterpret_cast<int*>(lds + LDS_CNT);
  asm volatile("s_waitcnt lgkmcnt(0)" : "+s"(ce), "+s"(cbc) :: "memory");
  const int npb = (((cbc <= 0 || cbc > LK) ? LK : cbc) + BS - 1) / BS;
  const int colp = npb - NPRE + (wave - 4);
  const bool vP = wave >= 4 && colp >= 0;
  unsigned long long bal[B];
  asm volatile("s_waitcnt vmcnt(5)" : "+v"(rowc), "+v"(key[0]), "+v"(key[1]), "+v"(key[2]), "+v"(key[3]), "+v"(key[4]), "+v"(key[5]), "+v"(key[6]), "+v"(key[7]) :: "memory");
  if (wave >= 4) {
    const int x01 = (int)((colp & 1) ? rowc[1] : rowc[0]), x23 = (int)((colp & 1) ? rowc[3] : rowc[2]);
    const int p = __builtin_amdgcn_readlane((colp & 2) ? x23 : x01, (colp >> 2) & 63);
    const int phP = p + ((p >> 31) & nphys);
    if (vP) {
      dma_tile(srdK, voff, (unsigned)phP * (unsigned)PAGEB + (unsigned)hc * ROWB, ring);
      dma_tile(srdV, voff, (unsigned)phP * (unsigned)PAGEB + (unsigned)hc * ROWB, ring + TILEB);
    }
    const int colq = colp - NPRE;
    const int y01 = (int)((colq & 1) ? rowc[1] : rowc[0]), y23 = (int)((colq & 1) ? rowc[3] : rowc[2]);
    const int pq = __builtin_amdgcn_readlane((colq & 2) ? y23 : y01, (colq >> 2) & 63);
    if (lane == 0) cnt[12 + (wave & 3)] = colq >= 0 ? pq + ((pq >> 31) & nphys) : -1;
#pragma unroll
    for (int b = 0; b < B; ++b) { bal[b] = 0ull; key[b] = -1; }
  } else {
    const int c0 = (colid + NOWN) * BS;
#pragma unroll
    for (int b = 0; b < B; ++b) {
      const int c = (ce[b] <= 0 || ce[b] > LK) ? LK : ce[b];
      const int p = key[b] + ((key[b] >> 31) & nphys);
      key[b] = (c0 < c) ? p : -1;
    }
    unsigned blo = 0, bhi = 0;
#pragma unroll
    for (int b = 0; b < B; ++b) {
      unsigned dm = (unsigned)(key[b] + 1);
#pragma unroll
      for (int b2 = 0; b2 < b; ++b2) dm = min(dm, (unsigned)(key[b] ^ key[b2]));
      bal[b] = __ballot(dm != 0u);
      asm("s_nop 1\n\tv_writelane_b32 %0, %2, %4\n\tv_writelane_b32 %1, %3, %4"
          : "+v"(blo), "+v"(bhi) : "s"((unsigned)bal[b]), "s"((unsigned)(bal[b] >> 32)), "i"(b));
    }
    if (lane < B) *reinterpret_cast<u32x2*>(lds + LDS_BAL + (lane * 4 + wave) * 8) = u32x2{blo, bhi};
    if (tid < 12) cnt[tid] = 0;
  }
  wait_lds();
  __builtin_amdgcn_s_barrier();
  const u32x2 gb = *reinterpret_cast<const u32x2*>(lds + LDS_BAL + (lane & 31) * 8);
  const int gown = __popc(gb[0]) + __popc(gb[1]);
  int incl = gown;
  incl = dpp_add<0x111>(incl); incl = dpp_add<0x112>(incl); incl = dpp_add<0x114>(incl); incl = dpp_add<0x118>(incl);
  incl = dpp_add<0x142, 0xa>(incl);
  const int excl = incl - gown;
  const int ntot = __builtin_amdgcn_readlane(incl, 31);
  const int nest = ntot + B * (NOWN - NPRE);
  auto bound = [&](int x, unsigned o) { return (int)(((unsigned)ntot * (unsigned)x + o) >> 5); };
  auto hoffs = [&](int k) { return (unsigned)((k + hg) & 3) * 8u + 4u; };
  const int lo = bound(s, 4u), nun = bound(s + 1, 28u) - lo;
  if (wave < 4) {
    int pos[B];
#pragma unroll
    for (int b = 0; b < B; ++b) {
      pos[b] = __builtin_amdgcn_readlane(excl, b * 4 + wave) + (int)__builtin_amdgcn_mbcnt_hi((unsigned)(bal[b] >> 32), __builtin_amdgcn_mbcnt_lo((unsigned)bal[b], 0u));
      const int rel = pos[b] - lo;
      if (((bal[b] >> lane) & 1ull) && (unsigned)rel < (unsigned)nun) {
        int mk = 0;
#pragma unroll
        for (int b2 = b; b2 < B; ++b2) mk |= (key[b2] == key[b]) ? (1 << (16 + b2)) : 0;
        *reinterpret_cast<int2*>(lds + LDS_LIST + rel * 8) = make_int2(key[b], colid | mk);
      }
    }
    int kc_ = key[0];
#pragma unroll
    for (int b = 1; b < B; ++b) if (b == bc) kc_ = key[b];
    int mp = 0;
#pragma unroll
    for (int b = B - 1; b >= 0; --b) if (key[b] == kc_) mp = pos[b];
    unsigned bit = 0;
    if (kc_ >= 0) {
      const int x = NSPL * (mp + 1) - 1 - (int)hoffs(s & 3);
      int sp = (int)((float)x * (1.0f / (float)ntot));
      const int r = x - sp * ntot;
      sp += (r >= ntot) ? 1 : 0; sp -= (r < 0) ? 1 : 0;
      bit = 1u << sp;
    }
    bit |= __builtin_amdgcn_update_dpp(0, bit, 0x111, 0xf, 0xf, false);
    bit |= __builtin_amdgcn_update_dpp(0, bit, 0x112, 0xf, 0xf, false);
    bit |= __builtin_amdgcn_update_dpp(0, bit, 0x114, 0xf, 0xf, false);
    bit |= __builtin_amdgcn_update_dpp(0, bit, 0x118, 0xf, 0xf, false);
    const unsigned all = __builtin_amdgcn_readlane(bit, 15) | __builtin_amdgcn_readlane(bit, 31) | __builtin_amdgcn_readlane(bit, 47) | __builtin_amdgcn_readlane(bit, 63);
    if (lane == 0) lds_or(cnt, all);
  }
  wait_lds();
  __builtin_amdgcn_s_barrier();

  unsigned kaddr[2], vaddr[4];
#pragma unroll
  for (int bb = 0; bb < 2; ++bb) kaddr[bb] = ring + c16 * 128 + (((4 * bb + g) ^ (c16 & 6)) * 16);
  {
    const int t = 4 * g + (c16 >> 2), p = lane & 3;
#pragma unroll
    for (int k = 0; k < 4; ++k) vaddr[k] = ring + t * 128 + ((((2 * k) ^ (t & 6)) + (p >> 1)) * 16) + (p & 1) * 8;
  }
  float m = -INFINITY, l = 0.f;
  f32x4 acc[6];
#pragma unroll
  for (int i = 0; i < 6; ++i) acc[i] = f32x4{0.f, 0.f, 0.f, 0.f};
  bf16x8 kf[6];
  u32x4 qf[6], vf[6], pf;
  auto read_k = [&](unsigned slot) {
#pragma unroll
    for (int u = 0; u < 6; ++u) kf[u] = *(const LAS bf16x8*)(size_t)(kaddr[u & 1] + slot + (u >> 1) * 2048);
  };
  auto read_v = [&](unsigned slot) {
#pragma unroll
    for (int u = 0; u < 6; ++u) {
      const s16x4 x = __builtin_amdgcn_ds_read_tr16_b64_v4i16((LAS s16x4*)(size_t)(vaddr[u & 3] + slot + (u >> 2) * 2048));
      const s16x4 y = __builtin_amdgcn_ds_read_tr16_b64_v4i16((LAS s16x4*)(size_t)(vaddr[(u + 6) & 3] + slot + ((u + 6) >> 2) * 2048));
      vf[u] = __builtin_bit_cast(u32x4, __builtin_shufflevector(x, y, 0, 1, 2, 3, 4, 5, 6, 7));
    }
  };
  auto scores = [&](int nval) {
    f32x4 s0 = {0.f, 0.f, 0.f, 0.f}, s1 = {0.f, 0.f, 0.f, 0.f};
#pragma unroll
    for (int u = 0; u < 6; u += 2) {
      s0 = __builtin_amdgcn_mfma_f32_16x16x32_bf16(kf[u], __builtin_bit_cast(bf16x8, qf[u]), s0, 0, 0, 0);
      s1 = __builtin_amdgcn_mfma_f32_16x16x32_bf16(kf[u + 1], __builtin_bit_cast(bf16x8, qf[u + 1]), s1, 0, 0, 0);
    }
    float sc[4];
#pragma unroll
    for (int r = 0; r < 4; ++r) sc[r] = (r < nval) ? (s0[r] + s1[r]) * SCALE_LOG2E : -INFINITY;
    float tm = fmaxf(fmaxf(sc[0], sc[1]), fmaxf(sc[2], sc[3]));
    tm = xrow16_max(tm);
    const float mn = fmaxf(m, tm);
    const float msub = (mn == -INFINITY) ? 0.f : mn;
    const float alpha = __builtin_amdgcn_exp2f(m - msub);
    float p[4];
#pragma unroll
    for (int r = 0; r < 4; ++r) p[r] = __builtin_amdgcn_exp2f(sc[r] - msub);
    l = l * alpha + ((p[0] + p[1]) + (p[2] + p[3]));
    if (__any(mn > m)) {
#pragma unroll
      for (int u = 0; u < 6; ++u) acc[u] *= alpha;
    }
    m = mn;
    const unsigned p01 = __builtin_bit_cast(unsigned, bf16x2{(__bf16)p[0], (__bf16)p[1]}), p23 = __builtin_bit_cast(unsigned, bf16x2{(__bf16)p[2], (__bf16)p[3]});
    pf = c16 < 8 ? u32x4{p01, p23, 0u, 0u} : u32x4{0u, 0u, p01, p23};
  };
  auto pv = [&]() {
#pragma unroll
    for (int u = 0; u < 6; ++u) acc[u] = __builtin_amdgcn_mfma_f32_16x16x32_bf16(__builtin_bit_cast(bf16x8, vf[u]), __builtin_bit_cast(bf16x8, pf), acc[u], 0, 0, 0);
  };
  const char* qs = lds + wave * RINGB + 2 * TILEB;
  const __amdgpu_buffer_rsrc_t srdR = __builtin_amdgcn_make_buffer_rsrc((void*)(ws + WS_REC), (short)0, (int)(WS_END - WS_REC), 0x00020000);
  const unsigned cntaddr = ldsbase + LDS_CNT;
  auto own_q = [&]() {
#pragma unroll
    for (int i = 0; i < 6; ++i) qf[i] = *reinterpret_cast<const u32x4*>(qs + B * ROWB + i * 64 + g * 16);
  };
  float* xp = reinterpret_cast<float*>(lds + LDS_PRE) + (wave ^ 4) * PROW;
  auto park_ml = [&]() {
    const float ls = xrow16_sum(l);
    if (lane == 0) *reinterpret_cast<f32x2*>(xp + D) = f32x2{m, ls};
  };
  auto park_acc = [&]() {
    if (cq == 0) {
#pragma unroll
      for (int u = 0; u < 6; ++u) *reinterpret_cast<f32x4*>(xp + 16 * (6 * (c16 >> 3) + u) + 4 * g) = acc[u];
    }
    wait_lds();
    asm volatile("s_mov_b64 exec, 1\n\tds_add_u32 %0, %1\n\ts_mov_b64 exec, -1" :: "v"(cntaddr + 8), "v"(1u) : "memory");
  };
  const int cx = (cbc <= 0 || cbc > LK) ? LK : cbc;

  if (wave < 4) {
    const unsigned oh = hoffs(hh);
    const int loh = bound(s, oh), np = bound(s + 1, oh) - loh;
    const int2 e0 = *reinterpret_cast<const int2*>(lds + LDS_LIST + (loh - lo) * 8), e1 = *reinterpret_cast<const int2*>(lds + LDS_LIST + (loh - lo) * 8 + 8);
    const int phL = __builtin_amdgcn_readfirstlane(cnt[12 + hh]);
    const bool vL = phL >= 0;
    const unsigned offL = (unsigned)phL * (unsigned)PAGEB + (unsigned)hc * ROWB;
    const int lb = loh - lo, npt = np + (vL ? 1 : 0), ntile = 2 * npt;
    int phA = __builtin_amdgcn_readfirstlane(e0.x), mtA = __builtin_amdgcn_readfirstlane(e0.y);
    int phB = __builtin_amdgcn_readfirstlane(e1.x), mtB = __builtin_amdgcn_readfirstlane(e1.y);
    if (npt > 0) {
      const unsigned offA = np > 0 ? (unsigned)phA * (unsigned)PAGEB + hoff : offL;
      dma_tile(srdK, voff, offA, ring);
      dma_tile(srdV, voff, offA, ring + TILEB);
    }
    if (npt > 0) wait_vm<13>(); else wait_vm<0>();
#pragma unroll
    for (int i = 0; i < 6; ++i) qf[i] = *reinterpret_cast<const u32x4*>(qs + cq * ROWB + i * 64 + g * 16);
    unsigned long long t64;
    asm volatile("s_memrealtime %6\n\ts_waitcnt lgkmcnt(0)" : "+v"(qf[0]), "+v"(qf[1]), "+v"(qf[2]), "+v"(qf[3]), "+v"(qf[4]), "+v"(qf[5]), "=s"(t64) :: "memory");
    int issued = npt > 0 ? 2 : 0;
    int myctx_raw = ce[0];
#pragma unroll
    for (int b = 1; b < B; ++b) myctx_raw = (cq == b) ? ce[b] : myctx_raw;
    const int myctx = (myctx_raw <= 0 || myctx_raw > LK) ? LK : myctx_raw;
    if (myctx_raw <= 0) {
#pragma unroll
      for (int i = 0; i < 6; ++i) qf[i] = u32x4{0u, 0u, 0u, 0u};
    }
    unsigned tlast = (unsigned)t64;
    const unsigned te0 = tlast + (unsigned)((nest * 83) >> 4);
    int ema = 83 * 16;
    auto post_pe = [&](unsigned pe) { asm volatile("s_mov_b64 exec, 1\n\tds_write_b32 %0, %1 offset:16\n\ts_mov_b64 exec, -1" :: "v"(cntaddr + hh * 4), "v"(pe) : "memory"); };
    if (npt == 0 || 12 == 0) post_pe(npt > 0 ? te0 : tlast);
    auto pace = [&](unsigned now, unsigned tev) {
      const float rinv = __builtin_amdgcn_rcpf((float)(ntile - issued));
      unsigned tnew;
      for (;;) {
        const unsigned te = tev ? tev : te0;
        const int left = (int)(te - tlast);
        const unsigned due = tlast + (left > 0 ? (unsigned)__builtin_amdgcn_readfirstlane((int)((float)left * rinv)) : 0u);
        const int d = (int)(now - due);
        if (d >= 0) { tnew = d < 20 ? due : now; break; }
        if (d < -40) __builtin_amdgcn_s_sleep(4); else __builtin_amdgcn_s_sleep(1);
        unsigned tv; unsigned long long tt;
        asm volatile("s_memrealtime %1\n\tds_read_b32 %0, %2 offset:32\n\ts_waitcnt lgkmcnt(0)" : "=v"(tv), "=s"(tt) : "v"(cntaddr) : "memory");
        now = (unsigned)tt; tev = __builtin_amdgcn_readfirstlane(tv);
      }
      if (issued >= 4) ema += ((int)(tnew - tlast) * 16 - ema) >> 2;
      tlast = tnew;
      ++issued;
    };
    auto clock_issue = [&](unsigned& tv, unsigned long long& tt) {
      asm volatile("s_memrealtime %1\n\tds_read_b32 %0, %2 offset:32" : "=v"(tv), "=s"(tt) : "v"(cntaddr) : "memory");
    };
    unsigned seen = 0;
    float lfin = 0.f;
    for (int n = 0; n < np; ++n) {
      const bool vM = n + 1 < np, vB = n + 1 < npt;
      const unsigned offB = vM ? (unsigned)phB * (unsigned)PAGEB + hoff : offL;
      seen |= (unsigned)mtA & 0xff0000u;
      wait_vm<6>();
      unsigned tv; unsigned long long tt;
      read_k(0u);
      clock_issue(tv, tt);
      int2 ec = *reinterpret_cast<const int2*>(lds + LDS_LIST + (lb + n + 2) * 8);
      asm volatile("s_waitcnt lgkmcnt(0)" : "+v"(kf[0]), "+v"(kf[1]), "+v"(kf[2]), "+v"(kf[3]), "+v"(kf[4]), "+v"(kf[5]), "+v"(tv), "+s"(tt), "+v"(ec.x), "+v"(ec.y) :: "memory");
      if (vB) {
        pace((unsigned)tt, __builtin_amdgcn_readfirstlane(tv));
        dma_tile(srdK, voff, offB, ring);
        if (issued >= 12) post_pe(tlast + (unsigned)(((ntile - issued) * ema) >> 4));
      }
      scores(((mtA >> (16 + cq)) & 1) ? (myctx - (mtA & 0xffff) * BS - 4 * g) : 0);
      const int phC = __builtin_amdgcn_readfirstlane(ec.x), mtC = __builtin_amdgcn_readfirstlane(ec.y);
      if (!vM) { lfin = xrow16_sum(l); asm volatile("" : "+v"(lfin)); }
      if (vB) wait_vm<6>(); else wait_vm<0>();
      read_v((unsigned)TILEB);
      clock_issue(tv, tt);
      asm volatile("s_waitcnt lgkmcnt(0)" : "+v"(vf[0]), "+v"(vf[1]), "+v"(vf[2]), "+v"(vf[3]), "+v"(vf[4]), "+v"(vf[5]), "+v"(tv), "+s"(tt) :: "memory");
      if (vB) {
        pace((unsigned)tt, __builtin_amdgcn_readfirstlane(tv));
        dma_tile(srdV, voff, offB, ring + TILEB);
        if (issued >= 12) post_pe(tlast + (unsigned)(((ntile - issued) * ema) >> 4));
      }
      if (!vM) {
        asm volatile("s_mov_b64 exec, 1\n\tds_add_u32 %0, %1 offset:12\n\ts_mov_b64 exec, -1" :: "v"(cntaddr), "v"(1u) : "memory");
      }
      pv();
      phA = phB; mtA = mtB; phB = phC; mtB = mtC;
    }
    if (np == 0) asm volatile("s_mov_b64 exec, 1\n\tds_add_u32 %0, %1 offset:12\n\ts_mov_b64 exec, -1" :: "v"(cntaddr), "v"(1u) : "memory");
    const unsigned wm = seen >> 16;
    const unsigned tag = *reinterpret_cast<const unsigned*>(lds + LDS_TAG + wave * 256 + (hh * 8 + cq) * 4) + 1u;
    float am = fmaxf(fabsf(acc[0][0]), fabsf(acc[0][1]));
#pragma unroll
    for (int t = 2; t < 24; t += 2) am = __builtin_fmaxf(__builtin_fmaxf(am, fabsf(acc[t >> 2][t & 3])), fabsf(acc[t >> 2][(t & 3) + 1]));
    const float inv = __builtin_amdgcn_rcpf(lfin);
    const unsigned lse = __float_as_uint(m + __builtin_amdgcn_logf(lfin));
    const int e = max(__builtin_amdgcn_frexp_expf(inv * am), -113);
    const unsigned tw = (tag << 16) | ((unsigned)(e + 127) << 8) | ((lse >> (8 * g)) & 0xffu);
    const float sa = __builtin_amdgcn_ldexpf(inv, 14 - e);
    unsigned y[12];
#pragma unroll
    for (int t = 0; t < 12; ++t) {
      const f32x2 v = {sa * acc[t >> 1][2 * (t & 1)], sa * acc[t >> 1][2 * (t & 1) + 1]};
      y[t] = __builtin_bit_cast(unsigned, __builtin_convertvector(v, f16x2));
    }
    {
      const int ro = ((wm >> cq) & 1u) ? ((h * B + cq) * NSPL + s) * RECB + ((c16 >> 3) * 4 + g) * 16 : 0x40000000;
#pragma unroll
      for (int jj = 0; jj < 4; ++jj)
        __builtin_amdgcn_raw_buffer_store_b128(u32x4{y[3 * jj], y[3 * jj + 1], y[3 * jj + 2], tw}, srdR, ro + jj * 128, 0, 16);
    }
    m = -INFINITY; l = 0.f;
#pragma unroll
    for (int i = 0; i < 6; ++i) acc[i] = f32x4{0.f, 0.f, 0.f, 0.f};
    if (vL) {
      own_q();
      wait_vm<10>();
      read_k(0u);
      asm volatile("s_waitcnt lgkmcnt(0)" : "+v"(kf[0]), "+v"(kf[1]), "+v"(kf[2]), "+v"(kf[3]), "+v"(kf[4]), "+v"(kf[5]),
                   "+v"(qf[0]), "+v"(qf[1]), "+v"(qf[2]), "+v"(qf[3]), "+v"(qf[4]), "+v"(qf[5]) :: "memory");
      if (cbc <= 0) {
#pragma unroll
        for (int i = 0; i < 6; ++i) qf[i] = u32x4{0u, 0u, 0u, 0u};
      }
      scores(BS);
      park_ml();
      wait_vm<4>();
      read_v((unsigned)TILEB);
      asm volatile("s_waitcnt lgkmcnt(0)" : "+v"(vf[0]), "+v"(vf[1]), "+v"(vf[2]), "+v"(vf[3]), "+v"(vf[4]), "+v"(vf[5]) :: "memory");
      pv();
    } else {
      park_ml();
    }
    park_acc();
    return;
  }

  if (vP) {
    wait_vm<13>();
    own_q();
    wait_vm<6>();
    read_k(0u);
    asm volatile("s_waitcnt lgkmcnt(0)" : "+v"(kf[0]), "+v"(kf[1]), "+v"(kf[2]), "+v"(kf[3]), "+v"(kf[4]), "+v"(kf[5]),
                 "+v"(qf[0]), "+v"(qf[1]), "+v"(qf[2]), "+v"(qf[3]), "+v"(qf[4]), "+v"(qf[5]) :: "memory");
    if (cbc <= 0) {
#pragma unroll
      for (int i = 0; i < 6; ++i) qf[i] = u32x4{0u, 0u, 0u, 0u};
    }
    scores(cx - colp * BS - 4 * g);
    wait_vm<0>();
    read_v((unsigned)TILEB);
    asm volatile("s_waitcnt lgkmcnt(0)" : "+v"(vf[0]), "+v"(vf[1]), "+v"(vf[2]), "+v"(vf[3]), "+v"(vf[4]), "+v"(vf[5]) :: "memory");
    pv();
  } else {
    wait_vm<0>();
  }
  park_ml();
  park_acc();
  const int cc = wave - 4, rr = lane >> 3;
  const unsigned touch = __builtin_amdgcn_readfirstlane(*reinterpret_cast<const volatile LAS unsigned*>((size_t)cntaddr));
  const unsigned ct = __builtin_amdgcn_readfirstlane(*reinterpret_cast<const volatile LAS unsigned*>((size_t)(ldsbase + LDS_TAG + wave * 256 + ((s & 3) * 8 + bc) * 4))) + 1u;
  int dk[3];
#pragma unroll
  for (int k = 0; k < 3; ++k) { const int t = 3 * cc + k; dk[k] = 16 * (6 * ((lane >> 2) & 1) + (t >> 1)) + 4 * (lane & 3) + 2 * (t & 1); }
  unsigned radr[4];
  unsigned long long tm[4];
#pragma unroll
  for (int j = 0; j < 4; ++j) {
    int n = 8 * j + rr, pos = 0;
    tm[j] = __ballot(n < __popc(touch));
#pragma unroll
    for (int w = 16; w >= 1; w >>= 1) { const int c = __popc((touch >> pos) & ((1u << w) - 1u)); if (n >= c) { n -= c; pos += w; } }
    radr[j] = (unsigned)(((hc * B + bc) * NSPL + (pos & 31)) * RECB + cc * 128 + (lane & 7) * 16);
  }
  const int r16 = lane >> 4;
  int dout = 2 * ((lane & 8) ? (r16 < 2 ? dk[1] : dk[2] + 1) : (r16 == 0 ? dk[0] : r16 == 1 ? dk[0] + 1 : r16 == 2 ? dk[1] + 1 : dk[2]));
  asm volatile("" : "+v"(radr[0]), "+v"(radr[1]), "+v"(radr[2]), "+v"(radr[3]), "+v"(dout));
  unsigned xa[4];
#pragma unroll
  for (int k = 0; k < 4; ++k) xa[k] = ldsbase + LDS_PRE + (unsigned)(rr * PROW + (k < 3 ? dk[k] : D)) * 4u;
  asm volatile("" : "+v"(xa[0]), "+v"(xa[1]), "+v"(xa[2]), "+v"(xa[3]));
  auto look = [&](unsigned& now, int& fin) {
    unsigned fv; unsigned long long tt;
    asm volatile("s_memrealtime %1\n\tds_read_b32 %0, %2 offset:12\n\ts_waitcnt lgkmcnt(0)" : "=v"(fv), "=s"(tt) : "v"(cntaddr) : "memory");
    now = (unsigned)tt; fin = (int)__builtin_amdgcn_readfirstlane(fv);
  };
  unsigned now; int fin;
  look(now, fin);
  const unsigned teI = now + (unsigned)((nest * 83) >> 4);
  if (wave == 7) {
    const __amdgpu_buffer_rsrc_t srdC = __builtin_amdgcn_make_buffer_rsrc((void*)(ws + WS_CTL), (short)0, NWG * 128, 0x00020000);
    const int peer = ((int)blockIdx.x + 17 * ((lane & 15) + 1)) & (NWG - 1);
    const unsigned tg0 = now;
    unsigned tecur = teI;
    unsigned tk = now >> 9;
    while (fin == 0) {
      if ((now >> 9) == tk) { __builtin_amdgcn_s_sleep(4); look(now, fin); continue; }
      tk = now >> 9;
      const unsigned tt = tk << 9;
      const u32x4 pw = *reinterpret_cast<const u32x4*>(lds + LDS_CNT + 16);
      const int pe = (int)((pw[0] - tt) + (pw[1] - tt) + (pw[2] - tt) + (pw[3] - tt)) >> 2;
      const bool started = pw[0] != 0u && pw[1] != 0u && pw[2] != 0u && pw[3] != 0u;
      if (started && pe < 300) continue;
      if (lane == 0 && started) __builtin_amdgcn_raw_buffer_store_b64(u32x2{tk, (unsigned)pe}, srdC, (int)blockIdx.x * 128, 0, 16);
      do { __builtin_amdgcn_s_sleep(2); look(now, fin); } while ((int)(now - tt) < 150 + 0 * (int)((blockIdx.x >> 3) & 15) && fin == 0);
      if (fin != 0) break;
      u32x2 v = {0u, 0u};
      if (lane < 16) v = __builtin_amdgcn_raw_buffer_load_b64(srdC, peer * 128, 0, 16);
      const bool ok = lane < 16 && v[0] == tk;
      int nn = ok ? 1 : 0, sum = ok ? (int)v[1] : 0;
      nn = dpp_add<0x111>(nn); nn = dpp_add<0x112>(nn); nn = dpp_add<0x114>(nn); nn = dpp_add<0x118>(nn);
      sum = dpp_add<0x111>(sum); sum = dpp_add<0x112>(sum); sum = dpp_add<0x114>(sum); sum = dpp_add<0x118>(sum);
      const int n16 = __builtin_amdgcn_readlane(nn, 15), s16 = __builtin_amdgcn_readlane(sum, 15);
      unsigned te = 0;
      if (1 && n16 >= 16 * 3 / 4) {
        const float mean = (float)s16 / (float)n16;
        int rel = (int)(mean * (1.0f - 4 * 0.001f));
        const int relcur = (int)(tecur - tt);
        if (0 && mean - (float)relcur < mean * (4 * 0.00025f)) rel = (int)((float)relcur * (1.0f - 0 * 0.001f));
        if (40) rel = min(rel, relcur + max((relcur * 40) / 1000, 30));
        const int span = (int)(tt + (unsigned)rel - tg0);
        if (span < ((nest * 83 * 3) >> 5) && span > ((nest * 83 * 3) >> 6)) {
          te = tt + (unsigned)rel;
          tecur = te;
          if (lane == 0) *reinterpret_cast<volatile LAS unsigned*>((size_t)(cntaddr + 32)) = te;
        }
      }
      look(now, fin);
    }
    asm volatile("s_waitcnt vmcnt(0)" ::: "memory");
  } else {
    while (fin == 0) { __builtin_amdgcn_s_sleep(4); look(now, fin); }
  }
  {
    const unsigned tgo = __builtin_amdgcn_readfirstlane(now) + 145;
    const __hip_bfloat16* orow = out + ((size_t)bc * H + hc) * D;
    const unsigned char* ep = ws + WS_EPOCH + (hc * B + bc) * 4;
#define PISSUE(b0, b1, b2, b3) \
        "v_cndmask_b32_e64 v142, v146, %[r0], s[68:69]\n\tv_cndmask_b32_e64 v143, v146, %[r1], s[70:71]\n\t" \
        "v_cndmask_b32_e64 v144, v146, %[r2], s[72:73]\n\tv_cndmask_b32_e64 v145, v146, %[r3], s[74:75]\n\t" \
        "buffer_load_dwordx4 v[" #b0 ":" #b0 "+3], v142, %[srdR], 0 offen sc1\n\tbuffer_load_dwordx4 v[" #b1 ":" #b1 "+3], v143, %[srdR], 0 offen sc1\n\t" \
        "buffer_load_dwordx4 v[" #b2 ":" #b2 "+3], v144, %[srdR], 0 offen sc1\n\tbuffer_load_dwordx4 v[" #b3 ":" #b3 "+3], v145, %[srdR], 0 offen sc1\n\t"
#define PTAKE(b, c, pend, cmp) \
        "s_and_b64 " cmp ", " pend ", " cmp "\n\ts_cbranch_scc1 71f\n\t" \
        "s_cmp_eq_u64 " pend ", 0\n\ts_cbranch_scc1 71f\n\t" \
        "v_mov_b64 v[" #c ":" #c "+1], v[" #b ":" #b "+1]\n\tv_mov_b64 v[" #c "+2:" #c "+3], v[" #b "+2:" #b "+3]\n\t" \
        "s_mov_b64 " pend ", 0\n71:\n\t"
#define PCHECK(b0, b1, b2, b3) \
        TRYROWS \
        "s_waitcnt vmcnt(4)\n\t" \
        "v_cmp_ne_u32_sdwa s[76:77], v[" #b0 "+3], s90 src0_sel:WORD_1 src1_sel:DWORD\n\t" \
        "v_cmp_ne_u32_sdwa s[78:79], v[" #b1 "+3], s90 src0_sel:WORD_1 src1_sel:DWORD\n\t" \
        "v_cmp_ne_u32_sdwa s[80:81], v[" #b2 "+3], s90 src0_sel:WORD_1 src1_sel:DWORD\n\t" \
        "v_cmp_ne_u32_sdwa s[82:83], v[" #b3 "+3], s90 src0_sel:WORD_1 src1_sel:DWORD\n\t" \
        "s_nop 1\n\t" \
        PTAKE(b0, 64, "s[68:69]", "s[76:77]") PTAKE(b1, 68, "s[70:71]", "s[78:79]") PTAKE(b2, 72, "s[72:73]", "s[80:81]") PTAKE(b3, 76, "s[74:75]", "s[82:83]") \
        "s_or_b64 s[76:77], s[68:69], s[70:71]\n\ts_or_b64 s[78:79], s[72:73], s[74:75]\n\ts_or_b64 s[76:77], s[76:77], s[78:79]\n\t" \
        "s_cbranch_scc0 39f\n\t"
#define TRYROWS \
        "s_cmp_lg_u32 s91, 0\n\ts_cbranch_scc1 72f\n\t" \
        "ds_read_b32 v98, %[cnt] offset:8\n\ts_waitcnt lgkmcnt(0)\n\tv_readfirstlane_b32 s89, v98\n\t" \
        "s_cmp_lt_u32 s89, 8\n\ts_cbranch_scc1 72f\n\t" \
        "ds_read_b64 v[96:97], %[x3]\n\tds_read_b64 v[104:105], %[x0]\n\tds_read_b64 v[106:107], %[x1]\n\tds_read_b64 v[108:109], %[x2]\n\t" \
        "s_mov_b32 s91, 1\n72:\n\t"
    asm volatile(
        "s_mov_b32 s91, 0\n\t"
        "s_and_b32 s90, %[ct], 0xffff\n\t"
        "s_movk_i32 s94, 0xff73\n\t"
        "s_mov_b32 s88, 0\n\t"
        "s_mov_b64 s[68:69], %[t0]\n\t"
        "s_mov_b64 s[70:71], %[t1]\n\t"
        "s_mov_b64 s[72:73], %[t2]\n\t"
        "s_mov_b64 s[74:75], %[t3]\n\t"
        "v_and_b32 v95, 3, %[lane]\n\t"
        "v_lshlrev_b32 v95, 3, v95\n\t"
        "v_mov_b32 v92, 0xff800000\n\t"
        ".irp r,64,66,68,70,72,74,76,78\n\t"
        "v_mov_b64 v[\\r:\\r+1], 0\n\t"
        ".endr\n"
        "23:\n\t"
        "s_memrealtime s[92:93]\n\t"
        "s_waitcnt lgkmcnt(0)\n\t"
        "s_sub_i32 s89, s92, %[tgo]\n\t"
        "s_cmp_lt_i32 s89, 0\n\t"
        "s_cbranch_scc0 10f\n\t"
        "s_sleep 1\n\t"
        "s_branch 23b\n"
        "10:\n\t"
        "s_mov_b64 exec, s[68:69]\n\ts_cbranch_execz 11f\n\t"
        "buffer_load_dwordx4 v[64:67], %[r0], %[srdR], 0 offen sc1\n"
        "11:\n\t"
        "s_mov_b64 exec, s[70:71]\n\ts_cbranch_execz 11f\n\t"
        "buffer_load_dwordx4 v[68:71], %[r1], %[srdR], 0 offen sc1\n"
        "11:\n\t"
        "s_mov_b64 exec, s[72:73]\n\ts_cbranch_execz 11f\n\t"
        "buffer_load_dwordx4 v[72:75], %[r2], %[srdR], 0 offen sc1\n"
        "11:\n\t"
        "s_mov_b64 exec, s[74:75]\n\ts_cbranch_execz 11f\n\t"
        "buffer_load_dwordx4 v[76:79], %[r3], %[srdR], 0 offen sc1\n"
        "11:\n\t"
        "s_mov_b64 exec, -1\n\t"
        TRYROWS
        "s_waitcnt vmcnt(0)\n\t"
        "v_cmp_ne_u32_sdwa s[76:77], v67, s90 src0_sel:WORD_1 src1_sel:DWORD\n\t"
        "v_cmp_ne_u32_sdwa s[78:79], v71, s90 src0_sel:WORD_1 src1_sel:DWORD\n\t"
        "v_cmp_ne_u32_sdwa s[80:81], v75, s90 src0_sel:WORD_1 src1_sel:DWORD\n\t"
        "v_cmp_ne_u32_sdwa s[82:83], v79, s90 src0_sel:WORD_1 src1_sel:DWORD\n\t"
        ".irp j,0,1,2,3\n\t"
        "v_lshlrev_b32_sdwa v[80+\\j], v95, v[67+4*\\j] dst_sel:DWORD dst_unused:UNUSED_PAD src0_sel:DWORD src1_sel:BYTE_0\n\t"
        ".endr\n\t"
        "s_and_b64 s[76:77], s[68:69], s[76:77]\n\t"
        "s_cselect_b64 s[68:69], %[t0], 0\n\t"
        "s_and_b64 s[78:79], s[70:71], s[78:79]\n\t"
        "s_cselect_b64 s[70:71], %[t1], 0\n\t"
        "s_and_b64 s[80:81], s[72:73], s[80:81]\n\t"
        "s_cselect_b64 s[72:73], %[t2], 0\n\t"
        "s_and_b64 s[82:83], s[74:75], s[82:83]\n\t"
        "s_cselect_b64 s[74:75], %[t3], 0\n\t"
        "s_or_b64 s[76:77], s[68:69], s[70:71]\n\t"
        "s_or_b64 s[78:79], s[72:73], s[74:75]\n\t"
        "s_or_b64 s[76:77], s[76:77], s[78:79]\n\t"
        "s_cbranch_scc0 40f\n\t"
        "v_mov_b32 v146, 0x40000000\n\t"
        "s_nop 0\n\t"
        PISSUE(110, 114, 118, 122)
        "s_sleep %[pdel]\n"
        "70:\n\t"
        PISSUE(126, 130, 134, 138)
        PCHECK(110, 114, 118, 122)
        PISSUE(110, 114, 118, 122)
        PCHECK(126, 130, 134, 138)
        "s_add_u32 s88, s88, 1\n\t"
        "s_cmp_le_u32 s88, 0x4000\n\t"
        "s_cbranch_scc1 70b\n"
        "39:\n\t"
        "s_nop 0\n\t"
        ".irp j,0,1,2,3\n\t"
        "v_lshlrev_b32_sdwa v[80+\\j], v95, v[67+4*\\j] dst_sel:DWORD dst_unused:UNUSED_PAD src0_sel:DWORD src1_sel:BYTE_0\n\t"
        ".endr\n\t"
        "s_nop 0\n"
        "40:\n\t"
        ".irp j,0,1,2,3\n\t"
        "v_or_b32_dpp v[80+\\j], v[80+\\j], v[80+\\j] quad_perm:[1,0,3,2] row_mask:0xf bank_mask:0xf\n\t"
        ".endr\n\t"
        ".irp j,0,1,2,3\n\t"
        "v_or_b32_dpp v[80+\\j], v[80+\\j], v[80+\\j] quad_perm:[2,3,0,1] row_mask:0xf bank_mask:0xf\n\t"
        ".endr\n\t"
        "v_cndmask_b32_e64 v80, v92, v80, %[t0]\n\t"
        "v_cndmask_b32_e64 v81, v92, v81, %[t1]\n\t"
        "v_cndmask_b32_e64 v82, v92, v82, %[t2]\n\t"
        "v_cndmask_b32_e64 v83, v92, v83, %[t3]\n\t"
        "v_max3_f32 v100, v80, v81, v82\n\t"
        "v_add_u32_sdwa v88, v67, s94 dst_sel:DWORD dst_unused:UNUSED_PAD src0_sel:BYTE_1 src1_sel:DWORD\n\t"
        "v_add_u32_sdwa v89, v71, s94 dst_sel:DWORD dst_unused:UNUSED_PAD src0_sel:BYTE_1 src1_sel:DWORD\n\t"
        "v_add_u32_sdwa v90, v75, s94 dst_sel:DWORD dst_unused:UNUSED_PAD src0_sel:BYTE_1 src1_sel:DWORD\n\t"
        "v_add_u32_sdwa v91, v79, s94 dst_sel:DWORD dst_unused:UNUSED_PAD src0_sel:BYTE_1 src1_sel:DWORD\n\t"
        "45:\n\t"
        TRYROWS
        "s_cmp_eq_u32 s91, 0\n\t"
        "s_cbranch_scc1 45b\n\t"
        "s_waitcnt lgkmcnt(0)\n\t"
        "v_max3_f32 v100, v100, v83, v96\n\t"
        "s_nop 1\n\t"
        "v_max_f32_dpp v100, v100, v100 row_ror:8 row_mask:0xf bank_mask:0xf\n\t"
        "v_mov_b32 v101, v100\n\t"
        "s_nop 1\n\t"
        "v_permlane16_swap_b32 v100, v101\n\t"
        "v_max_f32 v100, v100, v101\n\t"
        "v_mov_b32 v101, v100\n\t"
        "s_nop 1\n\t"
        "v_permlane32_swap_b32 v100, v101\n\t"
        "v_max_f32 v100, v100, v101\n\t"
        "v_sub_f32 v102, v96, v100\n\t"
        "v_sub_f32 v84, v80, v100\n\t"
        "v_sub_f32 v85, v81, v100\n\t"
        "v_sub_f32 v86, v82, v100\n\t"
        "v_sub_f32 v87, v83, v100\n\t"
        "v_exp_f32 v102, v102\n\t"
        "v_exp_f32 v84, v84\n\t"
        "v_exp_f32 v85, v85\n\t"
        "v_exp_f32 v86, v86\n\t"
        "v_exp_f32 v87, v87\n\t"
        "v_fma_f32 v97, v97, v102, v84\n\t"
        "v_mul_f32 v104, v104, v102\n\t"
        "v_add_f32 v103, v85, v86\n\t"
        "v_mul_f32 v105, v105, v102\n\t"
        "v_add_f32 v97, v97, v87\n\t"
        "v_mul_f32 v106, v106, v102\n\t"
        "v_add_f32 v97, v97, v103\n\t"
        "v_mul_f32 v107, v107, v102\n\t"
        "v_ldexp_f32 v84, v84, v88\n\t"
        "v_mul_f32 v108, v108, v102\n\t"
        "v_ldexp_f32 v85, v85, v89\n\t"
        "v_mul_f32 v109, v109, v102\n\t"
        "v_ldexp_f32 v86, v86, v90\n\t"
        "v_add_f32_dpp v102, v97, v97 row_ror:8 row_mask:0xf bank_mask:0xf\n\t"
        "v_ldexp_f32 v87, v87, v91\n\t"
        "v_mov_b32 v103, v102\n\t"
        "v_fma_mix_f32 v104, v64, v84, v104 op_sel:[0,0,0] op_sel_hi:[1,0,0]\n\t"
        "v_fma_mix_f32 v105, v64, v84, v105 op_sel:[1,0,0] op_sel_hi:[1,0,0]\n\t"
        "v_fma_mix_f32 v106, v65, v84, v106 op_sel:[0,0,0] op_sel_hi:[1,0,0]\n\t"
        "v_fma_mix_f32 v107, v65, v84, v107 op_sel:[1,0,0] op_sel_hi:[1,0,0]\n\t"
        "v_fma_mix_f32 v108, v66, v84, v108 op_sel:[0,0,0] op_sel_hi:[1,0,0]\n\t"
        "v_fma_mix_f32 v109, v66, v84, v109 op_sel:[1,0,0] op_sel_hi:[1,0,0]\n\t"
        "s_cmp_eq_u64 %[t1], 0\n\ts_cbranch_scc1 50f\n\t"
        "v_fma_mix_f32 v104, v68, v85, v104 op_sel:[0,0,0] op_sel_hi:[1,0,0]\n\t"
        "v_fma_mix_f32 v105, v68, v85, v105 op_sel:[1,0,0] op_sel_hi:[1,0,0]\n\t"
        "v_fma_mix_f32 v106, v69, v85, v106 op_sel:[0,0,0] op_sel_hi:[1,0,0]\n\t"
        "v_fma_mix_f32 v107, v69, v85, v107 op_sel:[1,0,0] op_sel_hi:[1,0,0]\n\t"
        "v_fma_mix_f32 v108, v70, v85, v108 op_sel:[0,0,0] op_sel_hi:[1,0,0]\n\t"
        "v_fma_mix_f32 v109, v70, v85, v109 op_sel:[1,0,0] op_sel_hi:[1,0,0]\n\t"
        "s_cmp_eq_u64 %[t2], 0\n\ts_cbranch_scc1 50f\n\t"
        "v_fma_mix_f32 v104, v72, v86, v104 op_sel:[0,0,0] op_sel_hi:[1,0,0]\n\t"
        "v_fma_mix_f32 v105, v72, v86, v105 op_sel:[1,0,0] op_sel_hi:[1,0,0]\n\t"
        "v_fma_mix_f32 v106, v73, v86, v106 op_sel:[0,0,0] op_sel_hi:[1,0,0]\n\t"
        "v_fma_mix_f32 v107, v73, v86, v107 op_sel:[1,0,0] op_sel_hi:[1,0,0]\n\t"
        "v_fma_mix_f32 v108, v74, v86, v108 op_sel:[0,0,0] op_sel_hi:[1,0,0]\n\t"
        "v_fma_mix_f32 v109, v74, v86, v109 op_sel:[1,0,0] op_sel_hi:[1,0,0]\n\t"
        "s_cmp_eq_u64 %[t3], 0\n\ts_cbranch_scc1 50f\n\t"
        "v_fma_mix_f32 v104, v76, v87, v104 op_sel:[0,0,0] op_sel_hi:[1,0,0]\n\t"
        "v_fma_mix_f32 v105, v76, v87, v105 op_sel:[1,0,0] op_sel_hi:[1,0,0]\n\t"
        "v_fma_mix_f32 v106, v77, v87, v106 op_sel:[0,0,0] op_sel_hi:[1,0,0]\n\t"
        "v_fma_mix_f32 v107, v77, v87, v107 op_sel:[1,0,0] op_sel_hi:[1,0,0]\n\t"
        "v_fma_mix_f32 v108, v78, v87, v108 op_sel:[0,0,0] op_sel_hi:[1,0,0]\n\t"
        "v_fma_mix_f32 v109, v78, v87, v109 op_sel:[1,0,0] op_sel_hi:[1,0,0]\n\t"
        "s_nop 0\n"
        "50:\n\t"
        "v_permlane16_swap_b32 v102, v103\n\t"
        "v_permlane32_swap_b32 v104, v107\n\t"
        "v_add_f32 v102, v102, v103\n\t"
        "v_permlane32_swap_b32 v105, v108\n\t"
        "v_mov_b32 v103, v102\n\t"
        "v_permlane32_swap_b32 v106, v109\n\t"
        "v_add_f32 v104, v104, v107\n\t"
        "v_add_f32 v105, v105, v108\n\t"
        "v_add_f32 v106, v106, v109\n\t"
        "v_permlane32_swap_b32 v102, v103\n\t"
        "v_mov_b32 v101, v106\n\t"
        "v_add_f32 v102, v102, v103\n\t"
        "v_permlane16_swap_b32 v104, v105\n\t"
        "v_rcp_f32 v102, v102\n\t"
        "v_permlane16_swap_b32 v106, v101\n\t"
        "v_add_f32 v104, v104, v105\n\t"
        "v_add_f32 v106, v106, v101\n\t"
        "v_and_b32 v103, 8, %[lane]\n\t"
        "v_cmp_ne_u32 vcc, 0, v103\n\t"
        "v_add_f32_dpp v104, v104, v104 row_ror:8 row_mask:0xf bank_mask:0xf\n\t"
        "v_add_f32_dpp v106, v106, v106 row_ror:8 row_mask:0xf bank_mask:0xf\n\t"
        "s_nop 0\n\t"
        "v_cndmask_b32 v103, v104, v106, vcc\n\t"
        "v_mul_f32 v103, v103, v102\n\t"
        "v_cvt_pk_bf16_f32 v103, v103, v103\n\t"
        "global_store_short %[dout], v103, %[orow] sc0 sc1\n\t"
        "s_cmp_lg_u32 %[cc], 0\n\t"
        "s_cbranch_scc1 41f\n\t"
        "s_mov_b64 exec, 1\n\t"
        "v_mov_b32 v100, 0\n\t"
        "v_mov_b32 v101, %[ct]\n\t"
        "global_store_dword v100, v101, %[ep] sc1\n\t"
        "s_mov_b64 exec, -1\n"
        "41:\n\t"
        "s_waitcnt vmcnt(0)"
        :
        : [lane] "v"(lane), [r0] "v"(radr[0]), [r1] "v"(radr[1]), [r2] "v"(radr[2]), [r3] "v"(radr[3]), [dout] "v"(dout),
          [t0] "s"(tm[0]), [t1] "s"(tm[1]), [t2] "s"(tm[2]), [t3] "s"(tm[3]), [ct] "s"(ct), [srdR] "s"(srdR), [tgo] "s"(tgo),
          [orow] "s"(orow), [ep] "s"(ep), [cc] "s"(cc), [psl] "i"(2), [pdel] "i"(8),
          [cnt] "v"(cntaddr), [x0] "v"(xa[0]), [x1] "v"(xa[1]), [x2] "v"(xa[2]), [x3] "v"(xa[3])
        : "memory", "scc", "vcc",
          "v64", "v65", "v66", "v67", "v68", "v69", "v70", "v71", "v72", "v73", "v74", "v75", "v76", "v77", "v78", "v79",
          "v80", "v81", "v82", "v83", "v84", "v85", "v86", "v87", "v88", "v89", "v90", "v91", "v92", "v95", "v96", "v97", "v98", "v99",
          "v100", "v101", "v102", "v103", "v104", "v105", "v106", "v107", "v108", "v109",
          "v110", "v111", "v112", "v113", "v114", "v115", "v116", "v117", "v118", "v119", "v120", "v121", "v122", "v123", "v124", "v125",
          "v126", "v127", "v128", "v129", "v130", "v131", "v132", "v133", "v134", "v135", "v136", "v137", "v138", "v139", "v140", "v141",
          "v142", "v143", "v144", "v145", "v146",
          "s68", "s69", "s70", "s71", "s72", "s73", "s74", "s75", "s76", "s77", "s78", "s79", "s80", "s81", "s82", "s83",
          "s88", "s89", "s90", "s91", "s92", "s93", "s94");
  }
}

extern "C" void kernel_launch(void* const* d_in, const int* in_sizes, int n_in, void* d_out, int out_size, void* d_ws, size_t ws_size,
                              hipStream_t stream) {
  const __hip_bfloat16* q = (const __hip_bfloat16*)d_in[0];
  const __hip_bfloat16* k = (const __hip_bfloat16*)d_in[1];
  const __hip_bfloat16* v = (const __hip_bfloat16*)d_in[2];
  const int* bt = (const int*)d_in[3];
  const int* ctx = (const int*)d_in[4];
  const int nphys = in_sizes[1] / (BS * H * D);
  attn_kernel<<<NWG, NTHR, 0, stream>>>(q, k, v, bt, ctx, (__hip_bfloat16*)d_out, (unsigned char*)d_ws, nphys);
}
```
